```python
import torch

def _softmax(x, axis=-1):
    return torch.softmax(x, dim=axis)
def _gelu(x):
    return torch.nn.functional.gelu(x, approximate="tanh")
def _silu(x):
    return torch.nn.functional.silu(x)
def _sigmoid(x):
    return torch.sigmoid(x)
def _layernorm(x, axis=-1, eps=1e-5):
    m = x.mean(dim=axis, keepdim=True)
    v = x.var(dim=axis, keepdim=True, unbiased=False)
    return (x - m) / torch.sqrt(v + eps)
def _scatter_add(tgt, idx, src, axis=0):
    return tgt.clone().index_add_(axis, idx, src)
def _segment_sum(src, seg, n):
    out = torch.zeros((n,) + src.shape[1:], dtype=src.dtype)
    return out.index_add_(0, seg, src)
def _pad(x, pw):
    flat = tuple(v for pair in reversed(pw) for v in pair)
    return torch.nn.functional.pad(x, flat)

kernel_name = 'sequential_preLN__gqa_block_cached__swiglu_ffn__decode'

def setup_inputs():

    _w = (lambda a: (lambda t: t.to(torch.bfloat16) if t.is_floating_point() else t)(torch.as_tensor(a)))
    def _A_setup():
        B, Sq, D = 2, 1, 512
        Hq, Hkv, Sk, Dh = 32, 8, 8192, 128
        assert Hq % Hkv == 0, (Hq, Hkv)
        return {
            "x": _w((lambda *s: torch.randn(*s, dtype=torch.bfloat16) * 0.05)(B, Sq, D)),
            "k_cache": _w((lambda *s: torch.randn(*s, dtype=torch.bfloat16) * 3.2)(B, Hkv, Sk, Dh)),
            "v_cache": _w((lambda *s: torch.randn(*s, dtype=torch.bfloat16) * 0.4)(B, Hkv, Sk, Dh)),
            "wq": _w((lambda *s: torch.randn(*s, dtype=torch.bfloat16) * 3.2)(D, Hq * Dh) * (D ** -0.5)),
            "wo": _w((lambda *s: torch.randn(*s, dtype=torch.bfloat16) * 0.4)(Hq * Dh, D) * ((Hq * Dh) ** -0.5)),
        }
    _A_in = _A_setup()

    def _B_setup():
        B, S, D, F = 2, 1, 512, 1792
        return {
            "x": _w((lambda *s: torch.randn(*s, dtype=torch.bfloat16) * 0.05)(B, S, D)),
            "w_gate": _w((lambda *s: torch.randn(*s, dtype=torch.bfloat16) * 0.05)(D, F)),
            "w_up": _w((lambda *s: torch.randn(*s, dtype=torch.bfloat16) * 0.05)(D, F)),
            "w_down": _w((lambda *s: torch.randn(*s, dtype=torch.bfloat16) * 0.05)(F, D)),
        }
    _B_in = _B_setup()

    return {
        "A_x": _A_in["x"],
        "A_k_cache": _A_in["k_cache"],
        "A_v_cache": _A_in["v_cache"],
        "A_wq": _A_in["wq"],
        "A_wo": _A_in["wo"],
        "B_x": _B_in["x"],
        "B_w_gate": _B_in["w_gate"],
        "B_w_up": _B_in["w_up"],
        "B_w_down": _B_in["w_down"],
    }

def reference(A_x, A_k_cache, A_v_cache, A_wq, A_wo, B_x, B_w_gate, B_w_up, B_w_down):
    def _A_ref(x, k_cache, v_cache, wq, wo):
        B, Sq, D = x.shape
        Hq, Dh = 32, 128
        Hkv = k_cache.shape[1]
        rep = Hq // Hkv
        q = (lambda x, *d: x.permute(*d))((x @ wq).reshape(B, Sq, Hq, Dh), 0, 2, 1, 3)
        k = (lambda x, n, axis: torch.repeat_interleave(x, n, dim=axis))(k_cache, rep, axis=1)
        v = (lambda x, n, axis: torch.repeat_interleave(x, n, dim=axis))(v_cache, rep, axis=1)
        scores = torch.einsum("bhqd,bhkd->bhqk", q, k) * (Dh ** -0.5)
        p = _softmax(scores, axis=-1)
        o = torch.einsum("bhqk,bhkd->bhqd", p, v)
        return (lambda x, *d: x.permute(*d))(o, 0, 2, 1, 3).reshape(B, Sq, Hq * Dh) @ wo

    def _B_ref(x, w_gate, w_up, w_down):
        g = _silu(x @ w_gate)
        u = x @ w_up
        return (g * u) @ w_down

    x = A_x
    h = x + _A_ref(_layernorm(x, axis=-1), A_k_cache, A_v_cache, A_wq, A_wo)
    h2 = h + _B_ref(_layernorm(h, axis=-1), B_w_gate, B_w_up, B_w_down)
    return h2
```

```cpp
#include <hip/hip_runtime.h>
#include <hip/hip_bf16.h>
#include <stdint.h>

#define EMU 0

typedef unsigned short u16;
typedef unsigned int u32;
typedef u32 u32x4 __attribute__((ext_vector_type(4)));

__device__ __forceinline__ float bf2f(u16 v) { return __uint_as_float(((u32)v) << 16); }
__device__ __forceinline__ float rbf(float f) {
  u32 u = __float_as_uint(f);
  u += 0x7FFFu + ((u >> 16) & 1u);
  return __uint_as_float(u & 0xFFFF0000u);
}
__device__ __forceinline__ float R(float f) {
  return f;
}
__device__ __forceinline__ u16 f2bf(float f) { return (u16)(__float_as_uint(rbf(f)) >> 16); }

constexpr int D = 512, HQ = 32, HKV = 8, SK = 8192, DH = 128, F = 1792, B = 2;
constexpr int NSPLIT = 16;

__device__ float block_sum(float v, float* red) {
  for (int o = 32; o > 0; o >>= 1) v += __shfl_xor(v, o);
  int w = threadIdx.x >> 6, nw = blockDim.x >> 6;
  __syncthreads();
  if ((threadIdx.x & 63) == 0) red[w] = v;
  __syncthreads();
  float s = 0;
  for (int i = 0; i < nw; i++) s += red[i];
  return s;
}

__device__ void ln512(float* v, float* red) {
  int t = threadIdx.x;
  float a = v[t], b = v[t + 256];
  float mean = block_sum(a + b, red) * (1.0f / D);
  float da = a - mean, db = b - mean;
  float var = block_sum(da * da + db * db, red) * (1.0f / D);
  float m = R(mean), vv = R(var);
  float den = R(sqrtf(R(vv + 1e-5f)));
  __syncthreads();
  v[t] = R(R(a - m) / den);
  v[t + 256] = R(R(b - m) / den);
  __syncthreads();
}

__global__ void k_q(const u16* x, const u16* wq, float* q) {
  __shared__ float xs[2][512];
  __shared__ float red[8];
  __shared__ float part[4][2][64];
  int t = threadIdx.x;
  for (int b = 0; b < 2; b++) {
    xs[b][t] = bf2f(x[b * D + t]);
    xs[b][t + 256] = bf2f(x[b * D + t + 256]);
  }
  __syncthreads();
  ln512(xs[0], red);
  ln512(xs[1], red);
  int c = blockIdx.x * 64 + (t & 63), ks = t >> 6;
  float a0 = 0, a1 = 0;
  for (int k = ks * 128; k < ks * 128 + 128; k++) {
    float w = bf2f(wq[(size_t)k * 4096 + c]);
    a0 += xs[0][k] * w;
    a1 += xs[1][k] * w;
  }
  part[ks][0][t & 63] = a0;
  part[ks][1][t & 63] = a1;
  __syncthreads();
  if (t < 128) {
    int b = t >> 6, cc = t & 63;
    float s = part[0][b][cc] + part[1][b][cc] + part[2][b][cc] + part[3][b][cc];
    q[b * 4096 + blockIdx.x * 64 + cc] = R(s);
  }
}

__global__ void __launch_bounds__(256) k_attn(const float* q, const u16* kc, const u16* vc, float* pm, float* pl, float* po) {
  __shared__ float qs[4][128];
  __shared__ float sc[4][512];
  __shared__ float red[8];
  __shared__ float oacc[2][4][128];
  int s = blockIdx.x, kvh = blockIdx.y, b = blockIdx.z, t = threadIdx.x;
  for (int i = t; i < 512; i += 256) qs[i >> 7][i & 127] = q[b * 4096 + kvh * 512 + i];
  __syncthreads();
  const u16* kb = kc + ((size_t)(b * HKV + kvh) * SK + s * 512) * DH;
  const u16* vb = vc + ((size_t)(b * HKV + kvh) * SK + s * 512) * DH;
#pragma unroll 1
  for (int key = t; key < 512; key += 256) {
    float a[4] = {0, 0, 0, 0};
#pragma unroll 1
    for (int d8 = 0; d8 < 16; d8++) {
      u32x4 kv = *(const u32x4*)(kb + (size_t)key * DH + d8 * 8);
      for (int j = 0; j < 4; j++) {
        float k0 = __uint_as_float(kv[j] << 16), k1 = __uint_as_float(kv[j] & 0xFFFF0000u);
        for (int h = 0; h < 4; h++) a[h] += qs[h][d8 * 8 + 2 * j] * k0 + qs[h][d8 * 8 + 2 * j + 1] * k1;
      }
    }
    for (int h = 0; h < 4; h++) sc[h][key] = R(R(a[h]) * 0.08838834764831845f);
  }
  __syncthreads();
  float mh[4], lh[4];
  for (int h = 0; h < 4; h++) {
    float v = fmaxf(sc[h][t], sc[h][t + 256]);
    for (int o = 32; o > 0; o >>= 1) v = fmaxf(v, __shfl_xor(v, o));
    __syncthreads();
    if ((t & 63) == 0) red[t >> 6] = v;
    __syncthreads();
    float m = fmaxf(fmaxf(red[0], red[1]), fmaxf(red[2], red[3]));
    float e0 = __expf(sc[h][t] - m), e1 = __expf(sc[h][t + 256] - m);
    float l = block_sum(e0 + e1, red);
    sc[h][t] = e0; sc[h][t + 256] = e1;
    mh[h] = m; lh[h] = l;
  }
  __syncthreads();
  int d = t & 127, half = t >> 7;
  float o[4] = {0, 0, 0, 0};
  for (int key = half * 256; key < half * 256 + 256; key++) {
    float v = bf2f(vb[(size_t)key * DH + d]);
    for (int h = 0; h < 4; h++) o[h] += sc[h][key] * v;
  }
  for (int h = 0; h < 4; h++) oacc[half][h][d] = o[h];
  __syncthreads();
  if (t < 128) {
    for (int h = 0; h < 4; h++) {
      int row = (b * HQ + kvh * 4 + h);
      po[((size_t)row * NSPLIT + s) * DH + d] = oacc[0][h][d] + oacc[1][h][d];
      if (t == 0) { pm[row * NSPLIT + s] = mh[h]; pl[row * NSPLIT + s] = lh[h]; }
    }
  }
}

__global__ void k_comb(const float* pm, const float* pl, const float* po, float* o) {
  int row = blockIdx.x, d = threadIdx.x;
  float M = -INFINITY;
  for (int s = 0; s < NSPLIT; s++) M = fmaxf(M, pm[row * NSPLIT + s]);
  float L = 0, acc = 0;
  for (int s = 0; s < NSPLIT; s++) {
    float w = __expf(pm[row * NSPLIT + s] - M);
    L += w * pl[row * NSPLIT + s];
    acc += w * po[((size_t)row * NSPLIT + s) * DH + d];
  }
  o[row * DH + d] = R(acc / L);
}

__global__ void k_wo(const u16* x, const float* o, const u16* wo, float* h) {
  __shared__ float part[32][2][8];
  int t = threadIdx.x, c = blockIdx.x * 8 + (t & 7), ks = t >> 3;
  float a0 = 0, a1 = 0;
  for (int k = ks * 128; k < ks * 128 + 128; k++) {
    float w = bf2f(wo[(size_t)k * D + c]);
    a0 += o[k] * w;
    a1 += o[4096 + k] * w;
  }
  part[ks][0][t & 7] = a0; part[ks][1][t & 7] = a1;
  __syncthreads();
  if (t < 16) {
    int b = t >> 3, cc = t & 7;
    float s = 0;
    for (int i = 0; i < 32; i++) s += part[i][b][cc];
    int col = blockIdx.x * 8 + cc;
    h[b * D + col] = R(bf2f(x[b * D + col]) + R(s));
  }
}

__global__ void k_gu(const float* h, const u16* wg, const u16* wu, float* act) {
  __shared__ float xs[2][512];
  __shared__ float red[8];
  __shared__ float part[8][4][32];
  int t = threadIdx.x;
  for (int b = 0; b < 2; b++) { xs[b][t] = h[b * D + t]; xs[b][t + 256] = h[b * D + t + 256]; }
  __syncthreads();
  ln512(xs[0], red);
  ln512(xs[1], red);
  int c = blockIdx.x * 32 + (t & 31), ks = t >> 5;
  float g0 = 0, g1 = 0, u0 = 0, u1 = 0;
  for (int k = ks * 64; k < ks * 64 + 64; k++) {
    float a = bf2f(wg[(size_t)k * F + c]), bb = bf2f(wu[(size_t)k * F + c]);
    g0 += xs[0][k] * a; g1 += xs[1][k] * a; u0 += xs[0][k] * bb; u1 += xs[1][k] * bb;
  }
  part[ks][0][t & 31] = g0; part[ks][1][t & 31] = g1; part[ks][2][t & 31] = u0; part[ks][3][t & 31] = u1;
  __syncthreads();
  if (t < 64) {
    int b = t >> 5, cc = t & 31;
    float g = 0, u = 0;
    for (int i = 0; i < 8; i++) { g += part[i][b][cc]; u += part[i][2 + b][cc]; }
    g = R(g); u = R(u);
    float sg = R(g / (1.0f + expf(-g)));
    act[b * F + blockIdx.x * 32 + cc] = R(sg * u);
  }
}

__global__ void k_dn(const float* h, const float* act, const u16* wd, u16* out) {
  __shared__ float part[32][2][8];
  int t = threadIdx.x, c = blockIdx.x * 8 + (t & 7), ks = t >> 3;
  float a0 = 0, a1 = 0;
  for (int k = ks * 56; k < ks * 56 + 56; k++) {
    float w = bf2f(wd[(size_t)k * D + c]);
    a0 += act[k] * w; a1 += act[F + k] * w;
  }
  part[ks][0][t & 7] = a0; part[ks][1][t & 7] = a1;
  __syncthreads();
  if (t < 16) {
    int b = t >> 3, cc = t & 7;
    float s = 0;
    for (int i = 0; i < 32; i++) s += part[i][b][cc];
    int col = blockIdx.x * 8 + cc;
    out[b * D + col] = f2bf(h[b * D + col] + R(s));
  }
}

extern "C" void kernel_launch(void* const* d_in, const int* in_sizes, int n_in, void* d_out, int out_size,
                              void* d_ws, size_t ws_size, hipStream_t stream) {
  const u16* x = (const u16*)d_in[0];
  const u16* kc = (const u16*)d_in[1];
  const u16* vc = (const u16*)d_in[2];
  const u16* wq = (const u16*)d_in[3];
  const u16* wo = (const u16*)d_in[4];
  const u16* wg = (const u16*)d_in[6];
  const u16* wu = (const u16*)d_in[7];
  const u16* wd = (const u16*)d_in[8];
  float* ws = (float*)d_ws;
  float* q = ws;
  float* pm = q + 8192;
  float* pl = pm + 1024;
  float* po = pl + 1024;
  float* o = po + 64 * 16 * 128;
  float* h = o + 8192;
  float* act = h + 1024;
  k_q<<<64, 256, 0, stream>>>(x, wq, q);
  k_attn<<<dim3(NSPLIT, HKV, B), 256, 0, stream>>>(q, kc, vc, pm, pl, po);
  k_comb<<<64, 128, 0, stream>>>(pm, pl, po, o);
  k_wo<<<64, 256, 0, stream>>>(x, o, wo, h);
  k_gu<<<56, 256, 0, stream>>>(h, wg, wu, act);
  k_dn<<<64, 256, 0, stream>>>(h, act, wd, (u16*)d_out);
}
```

```cpp
#include <hip/hip_runtime.h>
#include <stdint.h>

typedef unsigned short u16;
typedef unsigned int u32;
typedef unsigned long long u64;
typedef u32 v4u __attribute__((ext_vector_type(4)));
typedef u32 v2u __attribute__((ext_vector_type(2)));
typedef float f4 __attribute__((ext_vector_type(4)));
typedef float f2 __attribute__((ext_vector_type(2)));
typedef short s4v __attribute__((ext_vector_type(4)));
typedef __bf16 bf8 __attribute__((ext_vector_type(8)));
typedef __bf16 bf2 __attribute__((ext_vector_type(2)));
#define GAS __attribute__((address_space(1)))
#define LAS __attribute__((address_space(3)))

#define DEBUG_TS 0
#define KD 8
#define VD 4
#define QSLEEP 40
#define WLATE 0
#define STSLEEP 8
#define STWAIT 1
#define WD1 3
#define KDF 16
#define VDF 8
#define KSLOW 2
#define WSTART 1
#define PSLEEP 0
#define ROTF 0
#define QSPREAD 1
#define QFAKE 4
#define PRET 1
#define CODE_KB 18

constexpr int DM = 512, DH = 128, FF = 1792;
constexpr int NWG = 256, NT = 1024;
constexpr int WS_EPOCH = 0;
constexpr int WS_FLAGS = 4096;
constexpr int WS_QG = 8192;
constexpr int WS_ST = WS_QG + 2 * 4 * 4096 * 8;
constexpr int WS_MB1 = WS_ST + 256 * 32;
constexpr int WS_MB2 = WS_MB1 + 256 * 2048;
constexpr int WS_MB3 = WS_MB2 + 2 * 16 * 2048;
constexpr int WS_QG2 = 2 << 20;
constexpr int WS_END = WS_QG2 + 9 * 262144;
constexpr int WS_DBG = (WS_END + 4095) / 4096 * 4096;
#define QADDR(off) (WS_QG2 + (off) + (((off) >> 12) & 7) * 262144)
constexpr u32 SENT = 0xFFFFFFFFu;
constexpr int AUX_SC1 = 16;
constexpr int OOB = 0x7FFFFFF0;
constexpr int SPIN_CAP = 1 << 16;
constexpr int L_KIMG = 0;
constexpr int L_WO = 0;
constexpr int L_GU = 0;
constexpr int L_DN = 65536;
constexpr int L_WQ = 131072;
constexpr int L_MRG = L_WQ;
constexpr int L_PFR = L_WQ + 16384;
constexpr int L_QPART = L_PFR + 4096;
constexpr int L_QS = L_QPART + 2048;
constexpr int L_OB = L_QS + 1024;
constexpr int L_ML = L_OB + 1024;
constexpr int L_FAC = L_ML + 256;
constexpr int L_ACT = L_FAC + 128;
constexpr int L_WT = L_ACT + 64;
constexpr int L_SYNC = L_WT + 512;
constexpr int L_DBG = L_SYNC + 128;
constexpr int L_DUMMY = L_DBG + 256;
constexpr int LDS_BYTES = L_DUMMY + 1024;
static_assert(LDS_BYTES <= 163840, "lds");
enum { C_QP = 0, C_Q = 1, C_ML = 2, F_FAC = 3, C_VISS = 4, C_W = 5, F_E = 6, F_FL = 7, F_P = 8  , W_E = 16  ,
       C_PUB = 18, C_O = 19, F_WT = 20, C_H = 21, C_A = 22, F_K = 24   };

struct Args {
  char* ws;
  const u16 *x, *wq, *k, *v, *wo, *wg, *wu, *wd;
  u16* out;
};

__device__ __forceinline__ float bflo(u32 v) { return __uint_as_float(v << 16); }
__device__ __forceinline__ float bfhi(u32 v) { return __uint_as_float(v & 0xFFFF0000u); }
__device__ __forceinline__ u32 cvtpk(float lo, float hi) {
  return __builtin_bit_cast(u32, __builtin_convertvector(f2{lo, hi}, bf2));
}
__device__ __forceinline__ float rbf(float f) { return bflo(cvtpk(f, f)); }
template <int CTRL> __device__ __forceinline__ float dpp(float x) {
  return __builtin_bit_cast(float, __builtin_amdgcn_mov_dpp(__builtin_bit_cast(int, x), CTRL, 0xf, 0xf, true));
}
constexpr int XOR1 = 0xB1, XOR2 = 0x4E, XOR7 = 0x141, XOR8 = 0x128, ROR4 = 0x124;
__device__ __forceinline__ float x16_sum(float x) {
  auto s = __builtin_amdgcn_permlane16_swap(__float_as_uint(x), __float_as_uint(x), false, false);
  return __uint_as_float(s[0]) + __uint_as_float(s[1]);
}
__device__ __forceinline__ float x32_sum(float x) {
  auto s = __builtin_amdgcn_permlane32_swap(__float_as_uint(x), __float_as_uint(x), false, false);
  return __uint_as_float(s[0]) + __uint_as_float(s[1]);
}
__device__ __forceinline__ float x16_max(float x) {
  auto s = __builtin_amdgcn_permlane16_swap(__float_as_uint(x), __float_as_uint(x), false, false);
  return fmaxf(__uint_as_float(s[0]), __uint_as_float(s[1]));
}
__device__ __forceinline__ float x32_max(float x) {
  auto s = __builtin_amdgcn_permlane32_swap(__float_as_uint(x), __float_as_uint(x), false, false);
  return fmaxf(__uint_as_float(s[0]), __uint_as_float(s[1]));
}
__device__ __forceinline__ float x4_get(float x) { return dpp<XOR7>(dpp<0x1B>(x)); }
__device__ __forceinline__ float wave_sum(float s) {
  s += dpp<XOR1>(s); s += dpp<XOR2>(s); s += dpp<XOR7>(s); s += dpp<XOR8>(s);
  s = x16_sum(s);
  return x32_sum(s);
}
__device__ __forceinline__ float rscatter(f4 v) {
  auto p = __builtin_amdgcn_permlane32_swap(__float_as_uint(v[0]), __float_as_uint(v[2]), false, false);
  auto q = __builtin_amdgcn_permlane32_swap(__float_as_uint(v[1]), __float_as_uint(v[3]), false, false);
  const float ps = __uint_as_float(p[0]) + __uint_as_float(p[1]);
  const float qs = __uint_as_float(q[0]) + __uint_as_float(q[1]);
  auto r = __builtin_amdgcn_permlane16_swap(__float_as_uint(ps), __float_as_uint(qs), false, false);
  return __uint_as_float(r[0]) + __uint_as_float(r[1]);
}
__device__ __forceinline__ f4 mfma16(v4u a, v4u b, f4 c) {
  return __builtin_amdgcn_mfma_f32_16x16x32_bf16(__builtin_bit_cast(bf8, a), __builtin_bit_cast(bf8, b), c, 0, 0, 0);
}
__device__ __forceinline__ v2u trread(LAS char* p) {
  s4v r = __builtin_amdgcn_ds_read_tr16_b64_v4i16((LAS s4v*)p);
  return __builtin_bit_cast(v2u, r);
}
__device__ __forceinline__ u32 canon(float f) {
  u32 u = __float_as_uint(f);
  return u == SENT ? 0x7FC00000u : u;
}
__device__ __forceinline__ v4u canon4(f4 v) { return v4u{canon(v[0]), canon(v[1]), canon(v[2]), canon(v[3])}; }
__device__ __forceinline__ bool valid4(v4u v) { return (v.x != SENT) & (v.y != SENT) & (v.z != SENT) & (v.w != SENT); }
#define LGKM0() asm volatile("s_waitcnt lgkmcnt(0)" ::: "memory")
#define VM0() asm volatile("s_waitcnt vmcnt(0)" ::: "memory")
#define CBAR() asm volatile("" ::: "memory")
#define OPAQUE(x) asm volatile("" : "+s"(x))
__device__ __forceinline__ void wg_barrier() {
  __builtin_amdgcn_sched_barrier(0);
  LGKM0();
  __builtin_amdgcn_s_barrier();
  CBAR();
  __builtin_amdgcn_sched_barrier(0);
}
__device__ __forceinline__ void lds_wait_ge(LAS u32* p, u32 want) {
  _Pragma("clang loop unroll(disable)") for (int spins = 0; spins < SPIN_CAP; ++spins) {
    u32 v = __hip_atomic_load(p, __ATOMIC_RELAXED, __HIP_MEMORY_SCOPE_WORKGROUP);
    if (__builtin_amdgcn_readfirstlane(v) >= want) break;
    __builtin_amdgcn_s_sleep(1);
  }
  CBAR();
}
__device__ __forceinline__ void lds_spin_ge(LAS u32* p, u32 want) {
  _Pragma("clang loop unroll(disable)") for (int spins = 0; spins < SPIN_CAP * 16; ++spins) {
    u32 v = __hip_atomic_load(p, __ATOMIC_RELAXED, __HIP_MEMORY_SCOPE_WORKGROUP);
    if (__builtin_amdgcn_readfirstlane(v) >= want) break;
  }
  CBAR();
}
__device__ __forceinline__ void lds_add(LAS u32* p, int lane) {
  LGKM0();
  if (lane == 0) __hip_atomic_fetch_add(p, 1u, __ATOMIC_RELAXED, __HIP_MEMORY_SCOPE_WORKGROUP);
}
__device__ __forceinline__ void lds_set(LAS u32* p, int lane) {
  LGKM0();
  if (lane == 0) __hip_atomic_store(p, 1u, __ATOMIC_RELAXED, __HIP_MEMORY_SCOPE_WORKGROUP);
}
template <int NTP> __device__ __forceinline__ void glds16(const void* gsrc, u32 lds_dst) {
  u32 keep;
  if (NTP)
    asm volatile("s_mov_b32 %0, m0\n\ts_mov_b32 m0, %2\n\ts_nop 0\n\tglobal_load_lds_dwordx4 %1, off nt\n\ts_mov_b32 m0, %0"
                 : "=&s"(keep) : "v"(gsrc), "s"(lds_dst) : "memory");
  else
    asm volatile("s_mov_b32 %0, m0\n\ts_mov_b32 m0, %2\n\ts_nop 0\n\tglobal_load_lds_dwordx4 %1, off\n\ts_mov_b32 m0, %0"
                 : "=&s"(keep) : "v"(gsrc), "s"(lds_dst) : "memory");
}
template <int NTP> __device__ __forceinline__ void dma_run2(__amdgpu_buffer_rsrc_t r, u32 va, u32 vb, u32 so, u32 gstep, u32 lds0, u32 npairs) {
  u32 keep;
  if (NTP)
    asm volatile(
        "s_nop 4\n\ts_mov_b32 %[keep], m0\n\ts_mov_b32 m0, %[l0]\n"
        "1:\n\ts_sub_u32 %[cnt], %[cnt], 1\n\t"
        "buffer_load_dwordx4 %[va], %[r], %[so] offen nt lds\n\t"
        "s_add_u32 m0, m0, 0x400\n\ts_add_u32 %[so], %[so], %[gs]\n\t"
        "buffer_load_dwordx4 %[vb], %[r], %[so] offen nt lds\n\t"
        "s_add_u32 m0, m0, 0x400\n\ts_add_u32 %[so], %[so], %[gs]\n\t"
        "s_cmp_lg_u32 %[cnt], 0\n\ts_cbranch_scc1 1b\n\ts_mov_b32 m0, %[keep]"
        : [keep] "=&s"(keep), [so] "+s"(so), [cnt] "+s"(npairs)
        : [va] "v"(va), [vb] "v"(vb), [r] "s"(r), [l0] "s"(lds0), [gs] "s"(gstep) : "memory", "scc");
  else
    asm volatile(
        "s_nop 4\n\ts_mov_b32 %[keep], m0\n\ts_mov_b32 m0, %[l0]\n"
        "1:\n\ts_sub_u32 %[cnt], %[cnt], 1\n\t"
        "buffer_load_dwordx4 %[va], %[r], %[so] offen lds\n\t"
        "s_add_u32 m0, m0, 0x400\n\ts_add_u32 %[so], %[so], %[gs]\n\t"
        "buffer_load_dwordx4 %[vb], %[r], %[so] offen lds\n\t"
        "s_add_u32 m0, m0, 0x400\n\ts_add_u32 %[so], %[so], %[gs]\n\t"
        "s_cmp_lg_u32 %[cnt], 0\n\ts_cbranch_scc1 1b\n\ts_mov_b32 m0, %[keep]"
        : [keep] "=&s"(keep), [so] "+s"(so), [cnt] "+s"(npairs)
        : [va] "v"(va), [vb] "v"(vb), [r] "s"(r), [l0] "s"(lds0), [gs] "s"(gstep) : "memory", "scc");
}
__device__ __forceinline__ void bl_lds(__amdgpu_buffer_rsrc_t r, u32 vo, u32 so, u32 lds) {
  u32 keep;
  asm volatile("s_mov_b32 %[keep], m0\n\ts_mov_b32 m0, %[l]\n\ts_nop 0\n\tbuffer_load_dwordx4 %[v], %[r], %[so] offen lds\n\ts_mov_b32 m0, %[keep]"
               : [keep] "=&s"(keep) : [v] "v"(vo), [r] "s"(r), [so] "s"(so), [l] "s"(lds) : "memory");
}
__device__ __forceinline__ __amdgpu_buffer_rsrc_t mkrsrc(const void* p, int bytes) {
  return __builtin_amdgcn_make_buffer_rsrc((void*)p, 0, bytes, 0x00020000);
}

#define STAMP(idx) do {} while (0)
#define FLUSH_TS() do {} while (0)

__device__ __forceinline__ void poll4(__amdgpu_buffer_rsrc_t rs, int o0, int o1, int o2, int o3, v4u& a0, v4u& a1, v4u& a2, v4u& a3) {
  a0 = __builtin_amdgcn_raw_buffer_load_b128(rs, o0, 0, AUX_SC1);
  a1 = __builtin_amdgcn_raw_buffer_load_b128(rs, o1, 0, AUX_SC1);
  a2 = __builtin_amdgcn_raw_buffer_load_b128(rs, o2, 0, AUX_SC1);
  a3 = __builtin_amdgcn_raw_buffer_load_b128(rs, o3, 0, AUX_SC1);
  _Pragma("clang loop unroll(disable)") for (int spins = 0; spins < SPIN_CAP; ++spins) {
    const bool v0 = valid4(a0), v1 = valid4(a1), v2 = valid4(a2), v3 = valid4(a3);
    if (__all(v0 && v1 && v2 && v3)) break;
    CBAR();
    if (PSLEEP) __builtin_amdgcn_s_sleep(PSLEEP);
    const v4u b0 = __builtin_amdgcn_raw_buffer_load_b128(rs, v0 ? OOB : o0, 0, AUX_SC1);
    const v4u b1 = __builtin_amdgcn_raw_buffer_load_b128(rs, v1 ? OOB : o1, 0, AUX_SC1);
    const v4u b2 = __builtin_amdgcn_raw_buffer_load_b128(rs, v2 ? OOB : o2, 0, AUX_SC1);
    const v4u b3 = __builtin_amdgcn_raw_buffer_load_b128(rs, v3 ? OOB : o3, 0, AUX_SC1);
    a0 = v0 ? a0 : b0; a1 = v1 ? a1 : b1; a2 = v2 ? a2 : b2; a3 = v3 ? a3 : b3;
  }
}
#define F4(x) __builtin_bit_cast(f4, x)

__global__ void __launch_bounds__(NT) fused_block(Args a) {
  __shared__ __attribute__((aligned(16))) char smem[LDS_BYTES];
  LAS char* L = (LAS char*)smem;
  LAS u32* SY = (LAS u32*)(L + L_SYNC);
  u64 pc0;
  asm volatile("s_getpc_b64 %0" : "=s"(pc0));
  const int tid = threadIdx.x, lane = tid & 63, wave = __builtin_amdgcn_readfirstlane(tid >> 6);
  const int w = blockIdx.x, x8 = w & 7, j = w >> 3;
  const int slot = x8 * 32 + j;
  const int g = lane >> 4, c = lane & 15;
  const int r8 = lane >> 3, ch = lane & 7;
  const __amdgpu_buffer_rsrc_t rs = mkrsrc(a.ws, WS_END);
  if (tid < 32) SY[tid] = 0;
  if (wave == 0) STAMP(0);
  int wv = wave;
  const u32 swA = (u32)((((ch >> 1) ^ ((r8 >> 1) & 1)) * 2 + (ch & 1)) * 16);
  const u32 swB = (u32)((((ch >> 1) ^ (((r8 >> 1) & 1) | 2)) * 2 + (ch & 1)) * 16);
  const u32 rot = ROTF;
  const int cb = w & 63, kq = w >> 6;
  v4u xs0, xs1, xf;
  if (wv < 4) {
    __builtin_amdgcn_s_setprio(3);
    const __amdgpu_buffer_rsrc_t rq = mkrsrc(a.wq, DM * 4096 * 2);
    const u32 vo = (u32)(r8 * 8192 + cb * 128);
    dma_run2<1>(rq, vo + swA, vo + swB, (u32)((128 * kq + 32 * wv) * 8192), 8 * 8192, (u32)(size_t)(L + L_WQ) + wv * 4096, 2);
    xs0 = *(const GAS v4u*)(a.x + lane * 8);
    xs1 = *(const GAS v4u*)(a.x + DM + lane * 8);
    xf = *(const GAS v4u*)(a.x + (c & 1) * DM + 128 * kq + 32 * wv + 8 * g);
    __builtin_amdgcn_sched_barrier(0);
  }
  OPAQUE(wv);
  if (wv > 8) {
    const char* cp = (const char*)(pc0 & ~(u64)255) + min((j & 3) * 7 + (wv - 9), CODE_KB - 1) * 1024 + lane * 16;
    glds16<0>(cp, (u32)(size_t)(L + L_DUMMY));
    if ((PRET & 1) && wv == 15) bl_lds(rs, (u32)QADDR((((lane >> 5) * 4 + kq) * 4096 + cb * 64 + (lane & 31) * 2) * 8), 0, (u32)(size_t)(L + L_DUMMY));
  }
  const bool isWO = j < 16;
  const int wi = 2 * x8 + ((j >> 3) & 1), wjj = j & 7;
  const int fidx = j - 16;
  const bool isFFN = (j >= 16) && (x8 < 4 || fidx < 12);
  const int fblk = (x8 < 4 ? x8 * 4 : 4 + x8 * 3) + (fidx >> 2);
  const int ff = fblk * 2 + ((fidx >> 1) & 1);
  const int fb = fidx & 1;
  const int co0 = WS_MB1 + slot * 2048 + lane * 16;
  const int o3_ = WS_MB3 + ((fb * 32 + (lane >> 2)) * 56 + ff) * 64 + (lane & 3) * 16;
  const int co2 = isFFN ? o3_ : (isWO && lane < 32 ? WS_MB2 + ((g * 16 + wi) * 512 + wjj * 64 + c * 4) * 4 : OOB);
  const int co3 = isFFN ? o3_ + 16 * 56 * 64 : OOB;
  const int co4 = lane < 2 ? WS_ST + slot * 32 + (lane & 1) * 16 : OOB;
#define CLEARS() do { const v4u sv = {SENT, SENT, SENT, SENT}; \
    __builtin_amdgcn_raw_buffer_store_b128(sv, rs, co0, 0, AUX_SC1); \
    __builtin_amdgcn_raw_buffer_store_b128(sv, rs, co0 + 1024, 0, AUX_SC1); \
    __builtin_amdgcn_raw_buffer_store_b128(sv, rs, co2, 0, AUX_SC1); \
    __builtin_amdgcn_raw_buffer_store_b128(sv, rs, co3, 0, AUX_SC1); \
    __builtin_amdgcn_raw_buffer_store_b128(sv, rs, co4, 0, AUX_SC1); } while (0)
  OPAQUE(wv);
  u64 E0 = 0;
  if (wv == 8) {
    __builtin_amdgcn_s_setprio(3);
    E0 = __hip_atomic_load((GAS u64*)(a.ws + WS_EPOCH), __ATOMIC_RELAXED, __HIP_MEMORY_SCOPE_AGENT);
    if (PRET & 2) {
      bl_lds(rs, (u32)co0, 0, (u32)(size_t)(L + L_DUMMY));
      bl_lds(rs, (u32)co0 + 1024, 0, (u32)(size_t)(L + L_DUMMY));
      bl_lds(rs, (u32)co2, 0, (u32)(size_t)(L + L_DUMMY));
      bl_lds(rs, (u32)co3, 0, (u32)(size_t)(L + L_DUMMY));
      bl_lds(rs, (u32)(lane == 2 ? WS_FLAGS + (w & ~1) * 8 : co4), 0, (u32)(size_t)(L + L_DUMMY));
    } else CLEARS();
    __builtin_amdgcn_sched_barrier(0);
  }
  wg_barrier();
  OPAQUE(wv);
  if (wv >= 4 && wv < 8) {
    __builtin_amdgcn_s_setprio(2);
    const int lam = wv - 4;
    const __amdgpu_buffer_rsrc_t rk = mkrsrc(a.k + (size_t)slot * 512 * DH, 131072);
    const u32 vo = (u32)(lam * 1024 + g * 256);
    const u32 v0 = vo + ((c ^ (4 * g + 0)) * 16), v1 = vo + ((c ^ (4 * g + 1)) * 16), v2 = vo + ((c ^ (4 * g + 2)) * 16), v3 = vo + ((c ^ (4 * g + 3)) * 16);
#define KPAIR(V) \
        "s_waitcnt vmcnt(%[kd])\n\t" \
        "buffer_load_dwordx4 " V ", %[rk], %[so] offen nt lds\n\t" \
        "s_add_u32 m0, m0, 0x10000\n\ts_add_u32 %[so], %[so], 0x1000\n\t" \
        "s_waitcnt vmcnt(%[kd])\n\t" \
        "buffer_load_dwordx4 " V ", %[rk], %[so] offen nt lds\n\t" \
        "s_sub_u32 m0, m0, 0xfc00\n\ts_add_u32 %[so], %[so], 0x1000\n\ts_and_b32 %[so], %[so], 0x1ffff\n\t"
#define KLOOP(DEPTH, IT0, NIT) do { u32 so = ((IT0) * 0x8000 + rot * 0x2000) & 0x1ffff, keep, cnt = NIT; \
    asm volatile( \
        "s_nop 4\n\ts_mov_b32 %[keep], m0\n\ts_mov_b32 m0, %[l0]\n" \
        "1:\n\ts_sub_u32 %[cnt], %[cnt], 1\n\t" \
        KPAIR("%[v0]") KPAIR("%[v1]") KPAIR("%[v2]") KPAIR("%[v3]") \
        "s_cmp_lg_u32 %[cnt], 0\n\ts_cbranch_scc1 1b\n\ts_mov_b32 m0, %[keep]" \
        : [keep] "=&s"(keep), [so] "+s"(so), [cnt] "+s"(cnt) \
        : [v0] "v"(v0), [v1] "v"(v1), [v2] "v"(v2), [v3] "v"(v3), [rk] "s"(rk), [l0] "s"((u32)(size_t)(L + L_KIMG) + (u32)lam * 16384u + (IT0) * 4096u), [kd] "n"((DEPTH) - 1) \
        : "memory", "scc"); } while (0)
    KLOOP(KD, 0, (isFFN ? KSLOW : 4));
    if (isFFN && KSLOW < 4) KLOOP(KDF, KSLOW, 4 - KSLOW);
    __builtin_amdgcn_s_setprio(3);
    if (lane == 0) __hip_atomic_store(SY + F_K + lam, 1u, __ATOMIC_RELAXED, __HIP_MEMORY_SCOPE_WORKGROUP);
    STAMP(10);
    asm volatile("s_waitcnt vmcnt(%0)" :: "n"(KD / 2) : "memory");
    if (lane == 0) __hip_atomic_store(SY + F_K + lam, 2u, __ATOMIC_RELAXED, __HIP_MEMORY_SCOPE_WORKGROUP);
    VM0();
    if (lane == 0) __hip_atomic_store(SY + F_K + lam, 3u, __ATOMIC_RELAXED, __HIP_MEMORY_SCOPE_WORKGROUP);
    STAMP(12);
  }
  OPAQUE(wv);
  if (wv < 4) {
    STAMP(25);
    float m0_, m1_, d0_, d1_;
    {
      float s0 = 0.f, s1 = 0.f;
#pragma unroll
      for (int e = 0; e < 4; e++) { s0 += bflo(xs0[e]) + bfhi(xs0[e]); s1 += bflo(xs1[e]) + bfhi(xs1[e]); }
      s0 = wave_sum(s0); s1 = wave_sum(s1);
      const float mean0 = s0 * (1.0f / DM), mean1 = s1 * (1.0f / DM);
      float q0 = 0.f, q1 = 0.f;
#pragma unroll
      for (int e = 0; e < 4; e++) {
        float t;
        t = bflo(xs0[e]) - mean0; q0 = fmaf(t, t, q0);
        t = bfhi(xs0[e]) - mean0; q0 = fmaf(t, t, q0);
        t = bflo(xs1[e]) - mean1; q1 = fmaf(t, t, q1);
        t = bfhi(xs1[e]) - mean1; q1 = fmaf(t, t, q1);
      }
      q0 = wave_sum(q0); q1 = wave_sum(q1);
      m0_ = mean0; m1_ = mean1; d0_ = q0; d1_ = q1;
    }
    const float mean = (c & 1) ? m1_ : m0_, qq = (c & 1) ? d1_ : d0_;
    const float m = rbf(mean), vv = rbf(qq * (1.0f / DM));
    const float den = rbf(sqrtf(rbf(vv + 1e-5f)));
    v4u af;
#pragma unroll
    for (int e = 0; e < 4; e++) af[e] = cvtpk(rbf(bflo(xf[e]) - m) / den, rbf(bfhi(xf[e]) - m) / den);
    STAMP(26);
    VM0();
    {
      const int r0_ = 8 * g + (c >> 2);
      const int sg0 = ((r0_ >> 1) & 1) | ((g & 1) << 1);
      LAS char* bp = L + L_WQ + wv * 4096 + r0_ * 128 + (c & 3) * 8;
      f4 d[4];
#pragma unroll
      for (int t = 0; t < 4; t++) {
        v2u b0 = trread(bp + ((t ^ sg0) * 32)), b1 = trread(bp + 512 + ((t ^ sg0) * 32));
        d[t] = mfma16(af, v4u{b0.x, b0.y, b1.x, b1.y}, f4{0.f, 0.f, 0.f, 0.f});
      }
      const f4 da = (g & 1) ? d[1] : d[0], db = (g & 1) ? d[3] : d[2];
      const f4 dd = (g & 2) ? db : da;
      *(LAS f2*)(L + L_QPART + wv * 512 + lane * 8) = f2{dd[0], dd[1]};
    }
    STAMP(27);
    lds_wait_ge(SY + F_E, 1);
    const u64 E = *(LAS u64*)(SY + W_E);
    const u32 tag = (u32)E + 1u;
    LGKM0();
    u32 cnt = 0;
    if (lane == 0) cnt = __hip_atomic_fetch_add(SY + C_QP, 1u, __ATOMIC_RELAXED, __HIP_MEMORY_SCOPE_WORKGROUP);
    cnt = __builtin_amdgcn_readfirstlane(cnt);
    CBAR();
    if (cnt == 3) {
      const int hb = lane >> 5, l32 = lane & 31;
      f4 sq = *(LAS f4*)(L + L_QPART + l32 * 16);
#pragma unroll
      for (int uu = 1; uu < 4; uu++) sq += *(LAS f4*)(L + L_QPART + uu * 512 + l32 * 16);
      const int qo = QADDR(((hb * 4 + kq) * 4096 + cb * 64 + l32 * 2) * 8);
      __builtin_amdgcn_raw_buffer_store_b128(v4u{__float_as_uint(hb ? sq[1] : sq[0]), tag, __float_as_uint(hb ? sq[3] : sq[2]), tag}, rs, qo, 0, AUX_SC1);
      STAMP(1);
    }
    {
      const int qoff = QADDR(((slot >> 7) * 4 * 4096 + ((slot >> 4) & 7) * 512 + wv * 128 + lane * 2) * 8);
      __builtin_amdgcn_s_sleep(QSLEEP);
      STAMP(28);
      v4u qg[4];
      _Pragma("clang loop unroll(disable)") for (int spins = 0; spins < SPIN_CAP; ++spins) {
        CBAR();
#pragma unroll
        for (int q = 0; q < 4; q++) { if (q < QFAKE) qg[q] = __builtin_amdgcn_raw_buffer_load_b128(rs, qoff + q * 4096 * 8, 0, AUX_SC1); else qg[q] = qg[0]; }
        bool ok = true;
#pragma unroll
        for (int q = 0; q < 4; q++) ok = ok & (qg[q].y == tag) & (qg[q].w == tag);
        if (__all(ok)) break;
      }
      const float qa = (__uint_as_float(qg[0].x) + __uint_as_float(qg[1].x)) + (__uint_as_float(qg[2].x) + __uint_as_float(qg[3].x));
      const float qb = (__uint_as_float(qg[0].z) + __uint_as_float(qg[1].z)) + (__uint_as_float(qg[2].z) + __uint_as_float(qg[3].z));
      *(LAS u32*)(L + L_QS + wv * 256 + lane * 4) = cvtpk(qa, qb);
      lds_add(SY + C_Q, lane);
      STAMP(29);
    }
    lds_wait_ge(SY + F_K + wv, 3);
  }
  OPAQUE(wv);
  if (wv == 8) {
    if (lane == 0) *(LAS u64*)(SY + W_E) = E0;
    lds_set(SY + F_E, lane);
    VM0();
    if (PRET & 2) { CLEARS(); VM0(); }
    if (lane == 0) __hip_atomic_store((GAS u64*)(a.ws + WS_FLAGS) + w, E0 + 1, __ATOMIC_RELAXED, __HIP_MEMORY_SCOPE_AGENT);
    STAMP(22);
  }
  OPAQUE(wv);
  if (wv >= 8) {
    const int u = wv - 8;
    __builtin_amdgcn_s_setprio(1);
    lds_wait_ge(SY + F_K + (u & 3), 1 + (u >> 2));
    v4u V[16];
    {
      const __amdgpu_buffer_rsrc_t rv = mkrsrc(a.v + (size_t)slot * 512 * DH, 131072);
      const int vo = u * 1024 + lane * 16;
#pragma unroll
      for (int t = 0; t < 16; t++) {
        if (t >= VD && !isFFN) asm volatile("s_waitcnt vmcnt(%0)" :: "n"(VD - 1) : "memory");
        if (t >= VDF && isFFN) asm volatile("s_waitcnt vmcnt(%0)" :: "n"(VDF - 1) : "memory");
        V[t] = __builtin_amdgcn_raw_buffer_load_b128(rv, vo, ((t + rot) & 15) * 8192, 2);
        __builtin_amdgcn_sched_barrier(0);
      }
    }
    __builtin_amdgcn_s_setprio(3);
    lds_add(SY + C_VISS, lane);
    STAMP(2);
    lds_wait_ge(SY + F_P + u, 1);
    f4 acc[8];
#pragma unroll
    for (int G = 0; G < 2; G++) {
      const v4u pf = *(LAS v4u*)(L + L_PFR + ((u * 2 + G) * 16 + g * 4 + (c & 3)) * 16);
#pragma unroll
      for (int d2 = 0; d2 < 4; d2++) {
        v4u lo, hi;
#pragma unroll
        for (int t2 = 0; t2 < 4; t2++) {
          const u32 va = V[8 * G + 2 * t2][d2], vb = V[8 * G + 2 * t2 + 1][d2];
          lo[t2] = __builtin_amdgcn_perm(vb, va, 0x05040100u);
          hi[t2] = __builtin_amdgcn_perm(vb, va, 0x07060302u);
        }
        if (G == 0) {
          acc[2 * d2] = mfma16(pf, lo, f4{0.f, 0.f, 0.f, 0.f});
          acc[2 * d2 + 1] = mfma16(pf, hi, f4{0.f, 0.f, 0.f, 0.f});
        } else {
          acc[2 * d2] = mfma16(pf, lo, acc[2 * d2]);
          acc[2 * d2 + 1] = mfma16(pf, hi, acc[2 * d2 + 1]);
        }
      }
      __builtin_amdgcn_sched_barrier(0);
    }
    STAMP(3);
    if (lane < 16) {
#pragma unroll
      for (int dd = 0; dd < 8; dd++) *(LAS f4*)(L + L_MRG + u * 2048 + dd * 256 + c * 16) = acc[dd];
    }
    lds_add(SY + C_O, lane);
    if (u < 2) {
      lds_wait_ge(SY + F_FAC, 1);
      f4 fac[8];
#pragma unroll
      for (int uu = 0; uu < 8; uu++) fac[uu] = *(LAS f4*)(L + L_FAC + uu * 16);
      lds_spin_ge(SY + C_O, 8);
      STAMP(20);
      f4 sacc = *(LAS f4*)(L + L_MRG + (u * 64 + lane) * 16) * fac[0];
#pragma unroll
      for (int uu = 1; uu < 8; uu++) sacc += *(LAS f4*)(L + L_MRG + uu * 2048 + (u * 64 + lane) * 16) * fac[uu];
      __builtin_amdgcn_raw_buffer_store_b128(canon4(sacc), rs, WS_MB1 + slot * 2048 + (u * 64 + lane) * 16, 0, AUX_SC1);
      lds_add(SY + C_PUB, lane);
      STAMP(4);
    }
    if (isWO) {
      const int bq = u >> 2;
      const int off = WS_MB1 + ((bq * 8 + x8) * 16 + g) * 2048 + (wi & 1) * 1024 + (u & 3) * 256 + c * 16;
      lds_wait_ge(SY + F_WT, 1);
      f4 wt[4];
#pragma unroll
      for (int i = 0; i < 4; i++) wt[i] = *(LAS f4*)(L + L_WT + (bq * 16 + g + 4 * i) * 16);
      v4u a0, a1, a2, a3;
      STAMP(31);
      poll4(rs, off, off + 4 * 2048, off + 8 * 2048, off + 12 * 2048, a0, a1, a2, a3);
      const float o = rscatter((F4(a0) * wt[0] + F4(a1) * wt[1]) + (F4(a2) * wt[2] + F4(a3) * wt[3]));
      *(LAS u16*)(L + L_OB + bq * 512 + ((u & 3) * 64 + c * 4 + g) * 2) = (u16)cvtpk(o, o);
      lds_add(SY + C_H, lane);
      STAMP(5);
      if (u == 0 && j < 8) {
        const int fi = x8 * 8 + j, bb = fi >> 5, tile = fi & 31;
        const int q4 = lane & 3, p16 = lane >> 2;
        const u32 xv = *(const GAS u16*)(a.x + bb * DM + tile * 16 + q4 * 4 + g);
        const int o2 = WS_MB2 + ((bb * 16 + p16) * 512 + tile * 16 + q4 * 4) * 4;
        v4u at;
        _Pragma("clang loop unroll(disable)") for (int spins = 0; spins < SPIN_CAP; ++spins) {
          CBAR();
          at = __builtin_amdgcn_raw_buffer_load_b128(rs, o2, 0, AUX_SC1);
          if (__all(valid4(at))) break;
        }
        float t = rscatter(F4(at));
        t += dpp<ROR4>(t);
        t += dpp<XOR8>(t);
        const float hv = rbf(bflo(xv) + rbf(t));
        const int o3 = WS_MB3 + (bb * 32 + tile) * 56 * 64 + lane * 16;
        const bool has3 = lane < 32;
        poll4(rs, o3, o3 + 1024, o3 + 2048, has3 ? o3 + 3072 : o3, a0, a1, a2, a3);
        f4 ds = (F4(a0) + F4(a1)) + F4(a2);
        if (has3) ds += F4(a3);
        t = rscatter(ds);
        t += dpp<ROR4>(t);
        t += dpp<XOR8>(t);
        const float ov = hv + rbf(t);
        if (c < 4) *(GAS u16*)(a.out + bb * DM + tile * 16 + q4 * 4 + g) = (u16)cvtpk(ov, ov);
        STAMP(9);
      }
    } else if (isFFN) {
      const u32 xv = *(const GAS u16*)(a.x + fb * DM + u * 64 + c * 4 + g);
      const int off = WS_MB2 + ((fb * 16 + g) * 512 + u * 64 + c * 4) * 4;
      lds_wait_ge(SY + F_FL, 1);
      lds_wait_ge(SY + C_W, 4);
      v4u bd[4];
      {
        const int r0_ = 8 * g + (c >> 2);
        const int t0_ = (r0_ & 3) | ((g & 1) << 2);
#pragma unroll
        for (int tt = 0; tt < 4; tt++) {
          const int ct = u * 4 + tt;
          v2u b0 = trread(L + L_DN + r0_ * 1024 + ((ct ^ t0_) * 32) + (c & 3) * 8);
          v2u b1 = trread(L + L_DN + (r0_ + 4) * 1024 + ((ct ^ t0_) * 32) + (c & 3) * 8);
          bd[tt] = v4u{b0.x, b0.y, b1.x, b1.y};
        }
      }
      v4u a0, a1, a2, a3;
      STAMP(30);
      poll4(rs, off, off + 4 * 2048, off + 8 * 2048, off + 12 * 2048, a0, a1, a2, a3);
      const float at = rscatter((F4(a0) + F4(a1)) + (F4(a2) + F4(a3)));
      const float hs = bflo(xv) + rbf(at);
      *(LAS u16*)(L + L_OB + (u * 64 + c * 4 + g) * 2) = (u16)cvtpk(hs, hs);
      lds_add(SY + C_H, lane);
      STAMP(6);
      lds_spin_ge(SY + C_A, 4);
      const v4u af = *(LAS v4u*)(L + L_ACT + g * 16);
      const int o3 = c == 0 ? WS_MB3 + ((fb * 32 + u * 4) * 56 + ff) * 64 + g * 16 : OOB;
#pragma unroll
      for (int tt = 0; tt < 4; tt++) {
        const f4 d = mfma16(bd[tt], af, f4{0.f, 0.f, 0.f, 0.f});
        __builtin_amdgcn_raw_buffer_store_b128(canon4(d), rs, o3, tt * 56 * 64, AUX_SC1);
      }
      STAMP(8);
    }
  }
  OPAQUE(wv);
  if (wv < 8) {
    const int u = wv;
    lds_wait_ge(SY + C_Q, 4);
    STAMP(11);
    {
      v4u qf[4];
#pragma unroll
      for (int kk = 0; kk < 4; kk++) qf[kk] = *(LAS v4u*)(L + L_QS + (c & 3) * 256 + (32 * kk + 8 * g) * 2);
      float sc[16];
#pragma unroll
      for (int T = 0; T < 4; T++) {
        f4 d = {0.f, 0.f, 0.f, 0.f};
        const int row = 16 * T + 4 * (c & 3) + (c >> 2);
#pragma unroll
        for (int kk = 0; kk < 4; kk++) {
          const v4u kf = *(LAS v4u*)(L + L_KIMG + (64 * u + row) * 256 + (((4 * kk + g) ^ c) * 16));
          d = mfma16(kf, qf[kk], d);
        }
#pragma unroll
        for (int i = 0; i < 4; i++) sc[4 * T + i] = rbf(rbf(d[i]) * 0.08838834764831845f);
      }
      float m = sc[0];
#pragma unroll
      for (int i = 1; i < 16; i++) m = fmaxf(m, sc[i]);
      m = x16_max(m);
      m = x32_max(m);
      float l = 0.f;
#pragma unroll
      for (int i = 0; i < 16; i++) { sc[i] = __builtin_amdgcn_exp2f((sc[i] - m) * 1.4426950408889634f); l += sc[i]; }
      l = x16_sum(l);
      l = x32_sum(l);
      if (c < 4) {
#pragma unroll
        for (int G = 0; G < 2; G++) {
          v4u pf = {cvtpk(sc[8 * G], sc[8 * G + 1]), cvtpk(sc[8 * G + 2], sc[8 * G + 3]), cvtpk(sc[8 * G + 4], sc[8 * G + 5]), cvtpk(sc[8 * G + 6], sc[8 * G + 7])};
          *(LAS v4u*)(L + L_PFR + ((u * 2 + G) * 16 + g * 4 + c) * 16) = pf;
        }
        if (g == 0) *(LAS f2*)(L + L_ML + (u * 4 + c) * 8) = f2{m, l};
      }
      lds_set(SY + F_P + u, lane);
      lds_add(SY + C_ML, lane);
    }
    STAMP(13);
    if (u == 1) {
      lds_wait_ge(SY + C_ML, 8);
      const int h = lane & 3, uu = (lane >> 2) & 7;
      const f2 ml = *(LAS f2*)(L + L_ML + (uu * 4 + h) * 8);
      float mw = ml[0];
      mw = fmaxf(mw, x4_get(mw)); mw = fmaxf(mw, dpp<XOR8>(mw)); mw = x16_max(mw);
      const float fac = __builtin_amdgcn_exp2f((ml[0] - mw) * 1.4426950408889634f);
      float lw = ml[1] * fac;
      lw += x4_get(lw); lw += dpp<XOR8>(lw); lw = x16_sum(lw);
      if (lane < 4) __builtin_amdgcn_raw_buffer_store_b64(v2u{canon(mw), canon(lw)}, rs, WS_ST + slot * 32 + lane * 8, 0, AUX_SC1);
      if (lane < 32) *(LAS float*)(L + L_FAC + lane * 4) = fac;
      lds_set(SY + F_FAC, lane);
      STAMP(14);
    }
    if (u == 0) {
      const u64 E = *(LAS u64*)(SY + W_E);
      const u32 elo = (u32)(E + 1), ehi = (u32)((E + 1) >> 32);
      _Pragma("clang loop unroll(disable)") for (int spins = 0; spins < SPIN_CAP; ++spins) {
        CBAR();
        const v4u f0 = __builtin_amdgcn_raw_buffer_load_b128(rs, WS_FLAGS + lane * 16, 0, AUX_SC1);
        const v4u f1 = __builtin_amdgcn_raw_buffer_load_b128(rs, WS_FLAGS + 1024 + lane * 16, 0, AUX_SC1);
        const bool ok = (f0.x == elo) & (f0.y == ehi) & (f0.z == elo) & (f0.w == ehi) & (f1.x == elo) & (f1.y == ehi) & (f1.z == elo) & (f1.w == ehi);
        if (__all(ok)) break;
      }
      if (w == 0 && lane == 0) __hip_atomic_store((GAS u64*)(a.ws + WS_EPOCH), E + 1, __ATOMIC_RELAXED, __HIP_MEMORY_SCOPE_AGENT);
      lds_set(SY + F_FL, lane);
      STAMP(17);
    }
    if (isWO) {
      if (u == 0) {
        v2u st0, st1;
        if (STWAIT) lds_wait_ge(SY + C_VISS, 8);
        const int soff = WS_ST + (x8 * 16) * 32 + lane * 8;
        _Pragma("clang loop unroll(disable)") for (int spins = 0; spins < SPIN_CAP; ++spins) {
          CBAR();
          st0 = __builtin_amdgcn_raw_buffer_load_b64(rs, soff, 0, AUX_SC1);
          st1 = __builtin_amdgcn_raw_buffer_load_b64(rs, soff + 128 * 32, 0, AUX_SC1);
          if (__all((st0.x != SENT) & (st0.y != SENT) & (st1.x != SENT) & (st1.y != SENT))) break;
          __builtin_amdgcn_s_sleep(STSLEEP);
        }
#pragma unroll
        for (int bb = 0; bb < 2; bb++) {
          const float ms = __uint_as_float(bb ? st1.x : st0.x), ls = __uint_as_float(bb ? st1.y : st0.y);
          float M = ms;
          M = fmaxf(M, x4_get(M)); M = fmaxf(M, dpp<XOR8>(M)); M = x16_max(M); M = x32_max(M);
          const float ex = __builtin_amdgcn_exp2f((ms - M) * 1.4426950408889634f);
          float Ls = ls * ex;
          Ls += x4_get(Ls); Ls += dpp<XOR8>(Ls); Ls = x16_sum(Ls); Ls = x32_sum(Ls);
          *(LAS float*)(L + L_WT + bb * 256 + lane * 4) = ex / Ls;
        }
        lds_set(SY + F_WT, lane);
        STAMP(18);
      }
      if (u >= 4) {
        lds_wait_ge(SY + C_ML, 8);
        if (WLATE & 1) lds_wait_ge(SY + C_PUB, 2); else lds_wait_ge(SY + C_VISS, 8);
        __builtin_amdgcn_s_setprio(0);
        const __amdgpu_buffer_rsrc_t rw = mkrsrc(a.wo, 4096 * DM * 2);
        const u32 vo = (u32)(((r8 & 3) * 128 + 8 * (r8 >> 2)) * 1024 + wjj * 128);
        dma_run2<1>(rw, vo + swA, vo + swB, (u32)((x8 * 512 + 4 * (wi & 1) + (u - 4)) * 1024), 16 * 1024, (u32)(size_t)(L + L_WO) + (u - 4) * 8192, 4);
        VM0();
        lds_add(SY + C_W, lane);
        STAMP(15);
      } else {
        lds_wait_ge(SY + C_W, 4);
        v4u bf[8];
        const int r0_ = 8 * g + (c >> 2);
        const int sg0 = ((r0_ >> 1) & 1) | ((g & 1) << 1);
        LAS char* bp = L + L_WO + r0_ * 128 + ((u ^ sg0) * 32) + (c & 3) * 8;
#pragma unroll
        for (int ks = 0; ks < 8; ks++) {
          v2u b0 = trread(bp + ks * 4096), b1 = trread(bp + ks * 4096 + 512);
          bf[ks] = v4u{b0.x, b0.y, b1.x, b1.y};
        }
        STAMP(19);
        lds_spin_ge(SY + C_H, 8);
        STAMP(21);
        f4 d = {0.f, 0.f, 0.f, 0.f};
#pragma unroll
        for (int ks = 0; ks < 8; ks++) {
          const v4u of = *(LAS v4u*)(L + L_OB + (c & 1) * 512 + (32 * ks + 8 * g) * 2);
          d = mfma16(bf[ks], of, d);
        }
        __builtin_amdgcn_raw_buffer_store_b128(canon4(d), rs, c < 2 ? WS_MB2 + ((c * 16 + wi) * 512 + wjj * 64 + u * 16 + g * 4) * 4 : OOB, 0, AUX_SC1);
        STAMP(16);
      }
    } else if (isFFN) {
      if (u >= 4) {
        lds_wait_ge(SY + C_ML, 8);
        if (WSTART) lds_wait_ge(SY + C_VISS, 8);
        __builtin_amdgcn_s_setprio(0);
        const int lam = u - 4;
        const __amdgpu_buffer_rsrc_t rg = mkrsrc(a.wg, DM * FF * 2), ru = mkrsrc(a.wu, DM * FF * 2), rd = mkrsrc(a.wd, FF * DM * 2);
        const int r16 = lane >> 2, q4 = lane & 3;
        const u32 vg = (u32)(r16 * FF * 2 + ff * 64 + ((q4 ^ (r16 >> 3)) * 16)), vu = (u32)(r16 * FF * 2 + ff * 64 + ((q4 ^ ((r16 >> 3) | 2)) * 16));
        u32 mode = 0;
#define PACE() do { if (mode == 0) { asm volatile("s_waitcnt vmcnt(%0)" :: "n"(WD1 - 1) : "memory"); \
                      mode = __builtin_amdgcn_readfirstlane(__hip_atomic_load(SY + C_PUB, __ATOMIC_RELAXED, __HIP_MEMORY_SCOPE_WORKGROUP)) >= 2; } } while (0)
        _Pragma("clang loop unroll(disable)") for (int t = 0; t < 8; t++) {
          const u32 P = lam * 8 + t;
          PACE();
          bl_lds(rg, vg, P * (16 * FF * 2), (u32)(size_t)(L + L_GU) + P * 1024);
          PACE();
          bl_lds(ru, vu, P * (16 * FF * 2), (u32)(size_t)(L + L_GU) + 32768 + P * 1024);
        }
        _Pragma("clang loop unroll(disable)") for (int t = 0; t < 8; t++) {
          const u32 k = lam * 8 + t;
          const u32 tau = (k & 3) | (((k >> 3) & 1) << 2);
          PACE();
          bl_lds(rd, (u32)((((lane >> 1) ^ tau) * 2 + (lane & 1)) * 16), (ff * 32 + k) * 1024, (u32)(size_t)(L + L_DN) + k * 1024);
        }
        VM0();
        lds_add(SY + C_W, lane);
        STAMP(15);
      } else {
        lds_wait_ge(SY + C_W, 4);
        v4u bfr[16];
        const int mu = (c >> 1) & 1;
        LAS char* bp = L + L_GU + mu * 32768 + (8 * g + (c >> 2)) * 64 + ((u ^ ((g & 1) | (mu << 1))) * 16) + (c & 1) * 8;
        f4 cs = {0.f, 0.f, 0.f, 0.f};
        const v4u ones = {0x3F803F80u, 0x3F803F80u, 0x3F803F80u, 0x3F803F80u};
#pragma unroll
        for (int ks = 0; ks < 16; ks++) {
          v2u b0 = trread(bp + ks * 2048), b1 = trread(bp + ks * 2048 + 256);
          bfr[ks] = v4u{b0.x, b0.y, b1.x, b1.y};
          cs = mfma16(ones, bfr[ks], cs);
        }
        const float colsum = cs[0];
        STAMP(23);
        lds_spin_ge(SY + C_H, 8);
        STAMP(24);
        const v4u hv = *(LAS v4u*)(L + L_OB + lane * 16);
        f4 d = {0.f, 0.f, 0.f, 0.f};
#pragma unroll
        for (int ks = 0; ks < 16; ks++) {
          const v4u af = *(LAS v4u*)(L + L_OB + (32 * ks + 8 * g) * 2);
          d = mfma16(af, bfr[ks], d);
        }
        float sm = 0.f;
#pragma unroll
        for (int e = 0; e < 4; e++) sm += bflo(hv[e]) + bfhi(hv[e]);
        const float mean = wave_sum(sm) * (1.0f / DM);
        float q2 = 0.f;
#pragma unroll
        for (int e = 0; e < 4; e++) {
          float t = bflo(hv[e]) - mean; q2 = fmaf(t, t, q2);
          t = bfhi(hv[e]) - mean; q2 = fmaf(t, t, q2);
        }
        q2 = wave_sum(q2);
        const float m = rbf(mean), vv = rbf(q2 * (1.0f / DM));
        const float den = rbf(sqrtf(rbf(vv + 1e-5f)));
        const float pre = rbf((d[0] - m * colsum) / den);
        const float other = dpp<XOR8>(pre);
        const float sg = rbf(pre / (1.0f + __expf(-pre)));
        const float act = sg * other;
        if (lane < 8) *(LAS u16*)(L + L_ACT + (u * 8 + lane) * 2) = (u16)cvtpk(act, act);
        lds_add(SY + C_A, lane);
        STAMP(7);
      }
    }
  }
  FLUSH_TS();
}

extern "C" void kernel_launch(void* const* d_in, const int* in_sizes, int n_in, void* d_out, int out_size,
                              void* d_ws, size_t ws_size, hipStream_t stream) {
  Args a;
  a.ws = (char*)d_ws;
  a.x = (const u16*)d_in[0];
  a.k = (const u16*)d_in[1];
  a.v = (const u16*)d_in[2];
  a.wq = (const u16*)d_in[3];
  a.wo = (const u16*)d_in[4];
  a.wg = (const u16*)d_in[6];
  a.wu = (const u16*)d_in[7];
  a.wd = (const u16*)d_in[8];
  a.out = (u16*)d_out;
  hipLaunchKernelGGL(fused_block, dim3(NWG), dim3(NT), 0, stream, a);
}
```

```cpp
#define PROBE 0
#include <hip/hip_runtime.h>
#include <stdint.h>

typedef unsigned short u16;
typedef unsigned int u32;
typedef unsigned long long u64;
typedef u32 v4u __attribute__((ext_vector_type(4)));
typedef u32 v2u __attribute__((ext_vector_type(2)));
typedef float f4 __attribute__((ext_vector_type(4)));
typedef float f2 __attribute__((ext_vector_type(2)));
typedef short s4v __attribute__((ext_vector_type(4)));
typedef __bf16 bf8 __attribute__((ext_vector_type(8)));
typedef __bf16 bf2 __attribute__((ext_vector_type(2)));
#define GAS __attribute__((address_space(1)))
#define LAS __attribute__((address_space(3)))

#define DEBUG_TS 0
#define KD 8
#define VD 4
#define QSLEEP 40
#define WLATE 0
#define STSLEEP 8
#define STWAIT 1
#define WD1 3
#define KDF 16
#define VDF 8
#define KSLOW 2
#define WSTART 1
#define PSLEEP 0
#define ROTF 0
#define QSPREAD 1
#define QFAKE 4
#define PRET 0
#define CODE_KB 18

constexpr int DM = 512, DH = 128, FF = 1792;
constexpr int NWG = 256, NT = 1024;
constexpr int WS_EPOCH = 0;
constexpr int WS_FLAGS = 4096;
constexpr int WS_QG = 8192;
constexpr int WS_ST = WS_QG + 2 * 4 * 4096 * 8;
constexpr int WS_MB1 = WS_ST + 256 * 32;
constexpr int WS_MB2 = WS_MB1 + 256 * 2048;
constexpr int WS_MB3 = WS_MB2 + 2 * 16 * 2048;
constexpr int WS_QG2 = 2 << 20;
constexpr int WS_END = WS_QG2 + 9 * 262144;
constexpr int WS_DBG = (WS_END + 4095) / 4096 * 4096;
#define QADDR(off) (WS_QG2 + (off) + (((off) >> 12) & 7) * 262144)
constexpr u32 SENT = 0xFFFFFFFFu;
constexpr int AUX_SC1 = 16;
constexpr int OOB = 0x7FFFFFF0;
constexpr int SPIN_CAP = 1 << 16;
constexpr int L_KIMG = 0;
constexpr int L_WO = 0;
constexpr int L_GU = 0;
constexpr int L_DN = 65536;
constexpr int L_WQ = 131072;
constexpr int L_MRG = L_WQ;
constexpr int L_PFR = L_WQ + 16384;
constexpr int L_QPART = L_PFR + 4096;
constexpr int L_QS = L_QPART + 2048;
constexpr int L_OB = L_QS + 1024;
constexpr int L_ML = L_OB + 1024;
constexpr int L_FAC = L_ML + 256;
constexpr int L_ACT = L_FAC + 128;
constexpr int L_WT = L_ACT + 64;
constexpr int L_SYNC = L_WT + 512;
constexpr int L_DBG = L_SYNC + 128;
constexpr int L_DUMMY = L_DBG + 256;
constexpr int LDS_BYTES = L_DUMMY + 1024;
static_assert(LDS_BYTES <= 163840, "lds");
enum { C_QP = 0, C_Q = 1, C_ML = 2, F_FAC = 3, C_VISS = 4, C_W = 5, F_E = 6, F_FL = 7, F_P = 8  , W_E = 16  ,
       C_PUB = 18, C_O = 19, F_WT = 20, C_H = 21, C_A = 22, F_K = 24   };

struct Args {
  char* ws;
  const u16 *x, *wq, *k, *v, *wo, *wg, *wu, *wd;
  u16* out;
};

__device__ __forceinline__ float bflo(u32 v) { return __uint_as_float(v << 16); }
__device__ __forceinline__ float bfhi(u32 v) { return __uint_as_float(v & 0xFFFF0000u); }
__device__ __forceinline__ u32 cvtpk(float lo, float hi) {
  return __builtin_bit_cast(u32, __builtin_convertvector(f2{lo, hi}, bf2));
}
__device__ __forceinline__ float rbf(float f) { return bflo(cvtpk(f, f)); }
template <int CTRL> __device__ __forceinline__ float dpp(float x) {
  return __builtin_bit_cast(float, __builtin_amdgcn_mov_dpp(__builtin_bit_cast(int, x), CTRL, 0xf, 0xf, true));
}
constexpr int XOR1 = 0xB1, XOR2 = 0x4E, XOR7 = 0x141, XOR8 = 0x128, ROR4 = 0x124;
__device__ __forceinline__ float x16_sum(float x) {
  auto s = __builtin_amdgcn_permlane16_swap(__float_as_uint(x), __float_as_uint(x), false, false);
  return __uint_as_float(s[0]) + __uint_as_float(s[1]);
}
__device__ __forceinline__ float x32_sum(float x) {
  auto s = __builtin_amdgcn_permlane32_swap(__float_as_uint(x), __float_as_uint(x), false, false);
  return __uint_as_float(s[0]) + __uint_as_float(s[1]);
}
__device__ __forceinline__ float x16_max(float x) {
  auto s = __builtin_amdgcn_permlane16_swap(__float_as_uint(x), __float_as_uint(x), false, false);
  return fmaxf(__uint_as_float(s[0]), __uint_as_float(s[1]));
}
__device__ __forceinline__ float x32_max(float x) {
  auto s = __builtin_amdgcn_permlane32_swap(__float_as_uint(x), __float_as_uint(x), false, false);
  return fmaxf(__uint_as_float(s[0]), __uint_as_float(s[1]));
}
__device__ __forceinline__ float x4_get(float x) { return dpp<XOR7>(dpp<0x1B>(x)); }
__device__ __forceinline__ float wave_sum(float s) {
  s += dpp<XOR1>(s); s += dpp<XOR2>(s); s += dpp<XOR7>(s); s += dpp<XOR8>(s);
  s = x16_sum(s);
  return x32_sum(s);
}
__device__ __forceinline__ float rscatter(f4 v) {
  auto p = __builtin_amdgcn_permlane32_swap(__float_as_uint(v[0]), __float_as_uint(v[2]), false, false);
  auto q = __builtin_amdgcn_permlane32_swap(__float_as_uint(v[1]), __float_as_uint(v[3]), false, false);
  const float ps = __uint_as_float(p[0]) + __uint_as_float(p[1]);
  const float qs = __uint_as_float(q[0]) + __uint_as_float(q[1]);
  auto r = __builtin_amdgcn_permlane16_swap(__float_as_uint(ps), __float_as_uint(qs), false, false);
  return __uint_as_float(r[0]) + __uint_as_float(r[1]);
}
__device__ __forceinline__ f4 mfma16(v4u a, v4u b, f4 c) {
  return __builtin_amdgcn_mfma_f32_16x16x32_bf16(__builtin_bit_cast(bf8, a), __builtin_bit_cast(bf8, b), c, 0, 0, 0);
}
__device__ __forceinline__ v2u trread(LAS char* p) {
  s4v r = __builtin_amdgcn_ds_read_tr16_b64_v4i16((LAS s4v*)p);
  return __builtin_bit_cast(v2u, r);
}
__device__ __forceinline__ u32 canon(float f) {
  u32 u = __float_as_uint(f);
  return u == SENT ? 0x7FC00000u : u;
}
__device__ __forceinline__ v4u canon4(f4 v) { return v4u{canon(v[0]), canon(v[1]), canon(v[2]), canon(v[3])}; }
__device__ __forceinline__ bool valid4(v4u v) { return (v.x != SENT) & (v.y != SENT) & (v.z != SENT) & (v.w != SENT); }
#define LGKM0() asm volatile("s_waitcnt lgkmcnt(0)" ::: "memory")
#define VM0() asm volatile("s_waitcnt vmcnt(0)" ::: "memory")
#define CBAR() asm volatile("" ::: "memory")
#define OPAQUE(x) asm volatile("" : "+s"(x))
__device__ __forceinline__ void wg_barrier() {
  __builtin_amdgcn_sched_barrier(0);
  LGKM0();
  __builtin_amdgcn_s_barrier();
  CBAR();
  __builtin_amdgcn_sched_barrier(0);
}
__device__ __forceinline__ void lds_wait_ge(LAS u32* p, u32 want) {
  _Pragma("clang loop unroll(disable)") for (int spins = 0; spins < SPIN_CAP; ++spins) {
    u32 v = __hip_atomic_load(p, __ATOMIC_RELAXED, __HIP_MEMORY_SCOPE_WORKGROUP);
    if (__builtin_amdgcn_readfirstlane(v) >= want) break;
    __builtin_amdgcn_s_sleep(1);
  }
  CBAR();
}
__device__ __forceinline__ void lds_spin_ge(LAS u32* p, u32 want) {
  _Pragma("clang loop unroll(disable)") for (int spins = 0; spins < SPIN_CAP * 16; ++spins) {
    u32 v = __hip_atomic_load(p, __ATOMIC_RELAXED, __HIP_MEMORY_SCOPE_WORKGROUP);
    if (__builtin_amdgcn_readfirstlane(v) >= want) break;
  }
  CBAR();
}
__device__ __forceinline__ void lds_add(LAS u32* p, int lane) {
  LGKM0();
  if (lane == 0) __hip_atomic_fetch_add(p, 1u, __ATOMIC_RELAXED, __HIP_MEMORY_SCOPE_WORKGROUP);
}
__device__ __forceinline__ void lds_set(LAS u32* p, int lane) {
  LGKM0();
  if (lane == 0) __hip_atomic_store(p, 1u, __ATOMIC_RELAXED, __HIP_MEMORY_SCOPE_WORKGROUP);
}
template <int NTP> __device__ __forceinline__ void glds16(const void* gsrc, u32 lds_dst) {
  u32 keep;
  if (NTP)
    asm volatile("s_mov_b32 %0, m0\n\ts_mov_b32 m0, %2\n\ts_nop 0\n\tglobal_load_lds_dwordx4 %1, off nt\n\ts_mov_b32 m0, %0"
                 : "=&s"(keep) : "v"(gsrc), "s"(lds_dst) : "memory");
  else
    asm volatile("s_mov_b32 %0, m0\n\ts_mov_b32 m0, %2\n\ts_nop 0\n\tglobal_load_lds_dwordx4 %1, off\n\ts_mov_b32 m0, %0"
                 : "=&s"(keep) : "v"(gsrc), "s"(lds_dst) : "memory");
}
template <int NTP> __device__ __forceinline__ void dma_run2(__amdgpu_buffer_rsrc_t r, u32 va, u32 vb, u32 so, u32 gstep, u32 lds0, u32 npairs) {
  u32 keep;
  if (NTP)
    asm volatile(
        "s_nop 4\n\ts_mov_b32 %[keep], m0\n\ts_mov_b32 m0, %[l0]\n"
        "1:\n\ts_sub_u32 %[cnt], %[cnt], 1\n\t"
        "buffer_load_dwordx4 %[va], %[r], %[so] offen nt lds\n\t"
        "s_add_u32 m0, m0, 0x400\n\ts_add_u32 %[so], %[so], %[gs]\n\t"
        "buffer_load_dwordx4 %[vb], %[r], %[so] offen nt lds\n\t"
        "s_add_u32 m0, m0, 0x400\n\ts_add_u32 %[so], %[so], %[gs]\n\t"
        "s_cmp_lg_u32 %[cnt], 0\n\ts_cbranch_scc1 1b\n\ts_mov_b32 m0, %[keep]"
        : [keep] "=&s"(keep), [so] "+s"(so), [cnt] "+s"(npairs)
        : [va] "v"(va), [vb] "v"(vb), [r] "s"(r), [l0] "s"(lds0), [gs] "s"(gstep) : "memory", "scc");
  else
    asm volatile(
        "s_nop 4\n\ts_mov_b32 %[keep], m0\n\ts_mov_b32 m0, %[l0]\n"
        "1:\n\ts_sub_u32 %[cnt], %[cnt], 1\n\t"
        "buffer_load_dwordx4 %[va], %[r], %[so] offen lds\n\t"
        "s_add_u32 m0, m0, 0x400\n\ts_add_u32 %[so], %[so], %[gs]\n\t"
        "buffer_load_dwordx4 %[vb], %[r], %[so] offen lds\n\t"
        "s_add_u32 m0, m0, 0x400\n\ts_add_u32 %[so], %[so], %[gs]\n\t"
        "s_cmp_lg_u32 %[cnt], 0\n\ts_cbranch_scc1 1b\n\ts_mov_b32 m0, %[keep]"
        : [keep] "=&s"(keep), [so] "+s"(so), [cnt] "+s"(npairs)
        : [va] "v"(va), [vb] "v"(vb), [r] "s"(r), [l0] "s"(lds0), [gs] "s"(gstep) : "memory", "scc");
}
__device__ __forceinline__ void bl_lds(__amdgpu_buffer_rsrc_t r, u32 vo, u32 so, u32 lds) {
  u32 keep;
  asm volatile("s_mov_b32 %[keep], m0\n\ts_mov_b32 m0, %[l]\n\ts_nop 0\n\tbuffer_load_dwordx4 %[v], %[r], %[so] offen lds\n\ts_mov_b32 m0, %[keep]"
               : [keep] "=&s"(keep) : [v] "v"(vo), [r] "s"(r), [so] "s"(so), [l] "s"(lds) : "memory");
}
__device__ __forceinline__ __amdgpu_buffer_rsrc_t mkrsrc(const void* p, int bytes) {
  return __builtin_amdgcn_make_buffer_rsrc((void*)p, 0, bytes, 0x00020000);
}

#define STAMP(idx) do { if ((idx) == PROBE) probe_t = __builtin_amdgcn_s_memrealtime(); } while (0)
#define FLUSH_TS() do { if (probe_t) { for (int i_ = 0; i_ < 100000; i_++) { if (__builtin_amdgcn_s_memrealtime() >= probe_t + 4000) break; __builtin_amdgcn_s_sleep(2); } } } while (0)

__device__ __forceinline__ void poll4(__amdgpu_buffer_rsrc_t rs, int o0, int o1, int o2, int o3, v4u& a0, v4u& a1, v4u& a2, v4u& a3) {
  a0 = __builtin_amdgcn_raw_buffer_load_b128(rs, o0, 0, AUX_SC1);
  a1 = __builtin_amdgcn_raw_buffer_load_b128(rs, o1, 0, AUX_SC1);
  a2 = __builtin_amdgcn_raw_buffer_load_b128(rs, o2, 0, AUX_SC1);
  a3 = __builtin_amdgcn_raw_buffer_load_b128(rs, o3, 0, AUX_SC1);
  _Pragma("clang loop unroll(disable)") for (int spins = 0; spins < SPIN_CAP; ++spins) {
    const bool v0 = valid4(a0), v1 = valid4(a1), v2 = valid4(a2), v3 = valid4(a3);
    if (__all(v0 && v1 && v2 && v3)) break;
    CBAR();
    if (PSLEEP) __builtin_amdgcn_s_sleep(PSLEEP);
    const v4u b0 = __builtin_amdgcn_raw_buffer_load_b128(rs, v0 ? OOB : o0, 0, AUX_SC1);
    const v4u b1 = __builtin_amdgcn_raw_buffer_load_b128(rs, v1 ? OOB : o1, 0, AUX_SC1);
    const v4u b2 = __builtin_amdgcn_raw_buffer_load_b128(rs, v2 ? OOB : o2, 0, AUX_SC1);
    const v4u b3 = __builtin_amdgcn_raw_buffer_load_b128(rs, v3 ? OOB : o3, 0, AUX_SC1);
    a0 = v0 ? a0 : b0; a1 = v1 ? a1 : b1; a2 = v2 ? a2 : b2; a3 = v3 ? a3 : b3;
  }
}
#define F4(x) __builtin_bit_cast(f4, x)

__global__ void __launch_bounds__(NT) fused_block(Args a) {
  __shared__ __attribute__((aligned(16))) char smem[LDS_BYTES];
  LAS char* L = (LAS char*)smem;
  LAS u32* SY = (LAS u32*)(L + L_SYNC);
  u64 pc0;
  asm volatile("s_getpc_b64 %0" : "=s"(pc0));
  const int tid = threadIdx.x, lane = tid & 63, wave = __builtin_amdgcn_readfirstlane(tid >> 6);
  const int w = blockIdx.x, x8 = w & 7, j = w >> 3;
  const int slot = x8 * 32 + j;
  const int g = lane >> 4, c = lane & 15;
  const int r8 = lane >> 3, ch = lane & 7;
  const __amdgpu_buffer_rsrc_t rs = mkrsrc(a.ws, WS_END);
  if (tid < 32) SY[tid] = 0;
  u64 probe_t = 0;
  if (wave == 0) STAMP(0);
  int wv = wave;
  const u32 swA = (u32)((((ch >> 1) ^ ((r8 >> 1) & 1)) * 2 + (ch & 1)) * 16);
  const u32 swB = (u32)((((ch >> 1) ^ (((r8 >> 1) & 1) | 2)) * 2 + (ch & 1)) * 16);
  const u32 rot = ROTF;
  const int cb = w & 63, kq = w >> 6;
  v4u xs0, xs1, xf;
  if (wv < 4) {
    __builtin_amdgcn_s_setprio(3);
    const __amdgpu_buffer_rsrc_t rq = mkrsrc(a.wq, DM * 4096 * 2);
    const u32 vo = (u32)(r8 * 8192 + cb * 128);
    dma_run2<1>(rq, vo + swA, vo + swB, (u32)((128 * kq + 32 * wv) * 8192), 8 * 8192, (u32)(size_t)(L + L_WQ) + wv * 4096, 2);
    xs0 = *(const GAS v4u*)(a.x + lane * 8);
    xs1 = *(const GAS v4u*)(a.x + DM + lane * 8);
    xf = *(const GAS v4u*)(a.x + (c & 1) * DM + 128 * kq + 32 * wv + 8 * g);
    __builtin_amdgcn_sched_barrier(0);
  }
  OPAQUE(wv);
  if (wv > 8) {
    const char* cp = (const char*)(pc0 & ~(u64)255) + min((j & 3) * 7 + (wv - 9), CODE_KB - 1) * 1024 + lane * 16;
    glds16<0>(cp, (u32)(size_t)(L + L_DUMMY));
    if ((PRET & 1) && wv == 15) bl_lds(rs, (u32)QADDR((((lane >> 5) * 4 + kq) * 4096 + cb * 64 + (lane & 31) * 2) * 8), 0, (u32)(size_t)(L + L_DUMMY));
  }
  const bool isWO = j < 16;
  const int wi = 2 * x8 + ((j >> 3) & 1), wjj = j & 7;
  const int fidx = j - 16;
  const bool isFFN = (j >= 16) && (x8 < 4 || fidx < 12);
  const int fblk = (x8 < 4 ? x8 * 4 : 4 + x8 * 3) + (fidx >> 2);
  const int ff = fblk * 2 + ((fidx >> 1) & 1);
  const int fb = fidx & 1;
  const int co0 = WS_MB1 + slot * 2048 + lane * 16;
  const int o3_ = WS_MB3 + ((fb * 32 + (lane >> 2)) * 56 + ff) * 64 + (lane & 3) * 16;
  const int co2 = isFFN ? o3_ : (isWO && lane < 32 ? WS_MB2 + ((g * 16 + wi) * 512 + wjj * 64 + c * 4) * 4 : OOB);
  const int co3 = isFFN ? o3_ + 16 * 56 * 64 : OOB;
  const int co4 = lane < 2 ? WS_ST + slot * 32 + (lane & 1) * 16 : OOB;
#define CLEARS() do { const v4u sv = {SENT, SENT, SENT, SENT}; \
    __builtin_amdgcn_raw_buffer_store_b128(sv, rs, co0, 0, AUX_SC1); \
    __builtin_amdgcn_raw_buffer_store_b128(sv, rs, co0 + 1024, 0, AUX_SC1); \
    __builtin_amdgcn_raw_buffer_store_b128(sv, rs, co2, 0, AUX_SC1); \
    __builtin_amdgcn_raw_buffer_store_b128(sv, rs, co3, 0, AUX_SC1); \
    __builtin_amdgcn_raw_buffer_store_b128(sv, rs, co4, 0, AUX_SC1); } while (0)
  OPAQUE(wv);
  u64 E0 = 0;
  if (wv == 8) {
    __builtin_amdgcn_s_setprio(3);
    E0 = __hip_atomic_load((GAS u64*)(a.ws + WS_EPOCH), __ATOMIC_RELAXED, __HIP_MEMORY_SCOPE_AGENT);
    if (PRET & 2) {
      bl_lds(rs, (u32)co0, 0, (u32)(size_t)(L + L_DUMMY));
      bl_lds(rs, (u32)co0 + 1024, 0, (u32)(size_t)(L + L_DUMMY));
      bl_lds(rs, (u32)co2, 0, (u32)(size_t)(L + L_DUMMY));
      bl_lds(rs, (u32)co3, 0, (u32)(size_t)(L + L_DUMMY));
      bl_lds(rs, (u32)(lane == 2 ? WS_FLAGS + (w & ~1) * 8 : co4), 0, (u32)(size_t)(L + L_DUMMY));
    } else CLEARS();
    __builtin_amdgcn_sched_barrier(0);
  }
  wg_barrier();
  OPAQUE(wv);
  if (wv >= 4 && wv < 8) {
    __builtin_amdgcn_s_setprio(2);
    const int lam = wv - 4;
    const __amdgpu_buffer_rsrc_t rk = mkrsrc(a.k + (size_t)slot * 512 * DH, 131072);
    const u32 vo = (u32)(lam * 1024 + g * 256);
    const u32 v0 = vo + ((c ^ (4 * g + 0)) * 16), v1 = vo + ((c ^ (4 * g + 1)) * 16), v2 = vo + ((c ^ (4 * g + 2)) * 16), v3 = vo + ((c ^ (4 * g + 3)) * 16);
#define KPAIR(V) \
        "s_waitcnt vmcnt(%[kd])\n\t" \
        "buffer_load_dwordx4 " V ", %[rk], %[so] offen nt lds\n\t" \
        "s_add_u32 m0, m0, 0x10000\n\ts_add_u32 %[so], %[so], 0x1000\n\t" \
        "s_waitcnt vmcnt(%[kd])\n\t" \
        "buffer_load_dwordx4 " V ", %[rk], %[so] offen nt lds\n\t" \
        "s_sub_u32 m0, m0, 0xfc00\n\ts_add_u32 %[so], %[so], 0x1000\n\ts_and_b32 %[so], %[so], 0x1ffff\n\t"
#define KLOOP(DEPTH, IT0, NIT) do { u32 so = ((IT0) * 0x8000 + rot * 0x2000) & 0x1ffff, keep, cnt = NIT; \
    asm volatile( \
        "s_nop 4\n\ts_mov_b32 %[keep], m0\n\ts_mov_b32 m0, %[l0]\n" \
        "1:\n\ts_sub_u32 %[cnt], %[cnt], 1\n\t" \
        KPAIR("%[v0]") KPAIR("%[v1]") KPAIR("%[v2]") KPAIR("%[v3]") \
        "s_cmp_lg_u32 %[cnt], 0\n\ts_cbranch_scc1 1b\n\ts_mov_b32 m0, %[keep]" \
        : [keep] "=&s"(keep), [so] "+s"(so), [cnt] "+s"(cnt) \
        : [v0] "v"(v0), [v1] "v"(v1), [v2] "v"(v2), [v3] "v"(v3), [rk] "s"(rk), [l0] "s"((u32)(size_t)(L + L_KIMG) + (u32)lam * 16384u + (IT0) * 4096u), [kd] "n"((DEPTH) - 1) \
        : "memory", "scc"); } while (0)
    KLOOP(KD, 0, (isFFN ? KSLOW : 4));
    if (isFFN && KSLOW < 4) KLOOP(KDF, KSLOW, 4 - KSLOW);
    __builtin_amdgcn_s_setprio(3);
    if (lane == 0) __hip_atomic_store(SY + F_K + lam, 1u, __ATOMIC_RELAXED, __HIP_MEMORY_SCOPE_WORKGROUP);
    STAMP(10);
    asm volatile("s_waitcnt vmcnt(%0)" :: "n"(KD / 2) : "memory");
    if (lane == 0) __hip_atomic_store(SY + F_K + lam, 2u, __ATOMIC_RELAXED, __HIP_MEMORY_SCOPE_WORKGROUP);
    VM0();
    if (lane == 0) __hip_atomic_store(SY + F_K + lam, 3u, __ATOMIC_RELAXED, __HIP_MEMORY_SCOPE_WORKGROUP);
    STAMP(12);
  }
  OPAQUE(wv);
  if (wv < 4) {
    STAMP(25);
    float m0_, m1_, d0_, d1_;
    {
      float s0 = 0.f, s1 = 0.f;
#pragma unroll
      for (int e = 0; e < 4; e++) { s0 += bflo(xs0[e]) + bfhi(xs0[e]); s1 += bflo(xs1[e]) + bfhi(xs1[e]); }
      s0 = wave_sum(s0); s1 = wave_sum(s1);
      const float mean0 = s0 * (1.0f / DM), mean1 = s1 * (1.0f / DM);
      float q0 = 0.f, q1 = 0.f;
#pragma unroll
      for (int e = 0; e < 4; e++) {
        float t;
        t = bflo(xs0[e]) - mean0; q0 = fmaf(t, t, q0);
        t = bfhi(xs0[e]) - mean0; q0 = fmaf(t, t, q0);
        t = bflo(xs1[e]) - mean1; q1 = fmaf(t, t, q1);
        t = bfhi(xs1[e]) - mean1; q1 = fmaf(t, t, q1);
      }
      q0 = wave_sum(q0); q1 = wave_sum(q1);
      m0_ = mean0; m1_ = mean1; d0_ = q0; d1_ = q1;
    }
    const float mean = (c & 1) ? m1_ : m0_, qq = (c & 1) ? d1_ : d0_;
    const float m = rbf(mean), vv = rbf(qq * (1.0f / DM));
    const float den = rbf(sqrtf(rbf(vv + 1e-5f)));
    v4u af;
#pragma unroll
    for (int e = 0; e < 4; e++) af[e] = cvtpk(rbf(bflo(xf[e]) - m) / den, rbf(bfhi(xf[e]) - m) / den);
    STAMP(26);
    VM0();
    {
      const int r0_ = 8 * g + (c >> 2);
      const int sg0 = ((r0_ >> 1) & 1) | ((g & 1) << 1);
      LAS char* bp = L + L_WQ + wv * 4096 + r0_ * 128 + (c & 3) * 8;
      f4 d[4];
#pragma unroll
      for (int t = 0; t < 4; t++) {
        v2u b0 = trread(bp + ((t ^ sg0) * 32)), b1 = trread(bp + 512 + ((t ^ sg0) * 32));
        d[t] = mfma16(af, v4u{b0.x, b0.y, b1.x, b1.y}, f4{0.f, 0.f, 0.f, 0.f});
      }
      const f4 da = (g & 1) ? d[1] : d[0], db = (g & 1) ? d[3] : d[2];
      const f4 dd = (g & 2) ? db : da;
      *(LAS f2*)(L + L_QPART + wv * 512 + lane * 8) = f2{dd[0], dd[1]};
    }
    STAMP(27);
    lds_wait_ge(SY + F_E, 1);
    const u64 E = *(LAS u64*)(SY + W_E);
    const u32 tag = (u32)E + 1u;
    LGKM0();
    u32 cnt = 0;
    if (lane == 0) cnt = __hip_atomic_fetch_add(SY + C_QP, 1u, __ATOMIC_RELAXED, __HIP_MEMORY_SCOPE_WORKGROUP);
    cnt = __builtin_amdgcn_readfirstlane(cnt);
    CBAR();
    if (cnt == 3) {
      const int hb = lane >> 5, l32 = lane & 31;
      f4 sq = *(LAS f4*)(L + L_QPART + l32 * 16);
#pragma unroll
      for (int uu = 1; uu < 4; uu++) sq += *(LAS f4*)(L + L_QPART + uu * 512 + l32 * 16);
      const int qo = QADDR(((hb * 4 + kq) * 4096 + cb * 64 + l32 * 2) * 8);
      __builtin_amdgcn_raw_buffer_store_b128(v4u{__float_as_uint(hb ? sq[1] : sq[0]), tag, __float_as_uint(hb ? sq[3] : sq[2]), tag}, rs, qo, 0, AUX_SC1);
      STAMP(1);
    }
    {
      const int qoff = QADDR(((slot >> 7) * 4 * 4096 + ((slot >> 4) & 7) * 512 + wv * 128 + lane * 2) * 8);
      __builtin_amdgcn_s_sleep(QSLEEP);
      STAMP(28);
      v4u qg[4];
      _Pragma("clang loop unroll(disable)") for (int spins = 0; spins < SPIN_CAP; ++spins) {
        CBAR();
#pragma unroll
        for (int q = 0; q < 4; q++) { if (q < QFAKE) qg[q] = __builtin_amdgcn_raw_buffer_load_b128(rs, qoff + q * 4096 * 8, 0, AUX_SC1); else qg[q] = qg[0]; }
        bool ok = true;
#pragma unroll
        for (int q = 0; q < 4; q++) ok = ok & (qg[q].y == tag) & (qg[q].w == tag);
        if (__all(ok)) break;
      }
      const float qa = (__uint_as_float(qg[0].x) + __uint_as_float(qg[1].x)) + (__uint_as_float(qg[2].x) + __uint_as_float(qg[3].x));
      const float qb = (__uint_as_float(qg[0].z) + __uint_as_float(qg[1].z)) + (__uint_as_float(qg[2].z) + __uint_as_float(qg[3].z));
      *(LAS u32*)(L + L_QS + wv * 256 + lane * 4) = cvtpk(qa, qb);
      lds_add(SY + C_Q, lane);
      STAMP(29);
    }
    lds_wait_ge(SY + F_K + wv, 3);
  }
  OPAQUE(wv);
  if (wv == 8) {
    if (lane == 0) *(LAS u64*)(SY + W_E) = E0;
    lds_set(SY + F_E, lane);
    VM0();
    if (PRET & 2) { CLEARS(); VM0(); }
    if (lane == 0) __hip_atomic_store((GAS u64*)(a.ws + WS_FLAGS) + w, E0 + 1, __ATOMIC_RELAXED, __HIP_MEMORY_SCOPE_AGENT);
    STAMP(22);
  }
  OPAQUE(wv);
  if (wv >= 8) {
    const int u = wv - 8;
    __builtin_amdgcn_s_setprio(1);
    lds_wait_ge(SY + F_K + (u & 3), 1 + (u >> 2));
    v4u V[16];
    {
      const __amdgpu_buffer_rsrc_t rv = mkrsrc(a.v + (size_t)slot * 512 * DH, 131072);
      const int vo = u * 1024 + lane * 16;
#pragma unroll
      for (int t = 0; t < 16; t++) {
        if (t >= VD && !isFFN) asm volatile("s_waitcnt vmcnt(%0)" :: "n"(VD - 1) : "memory");
        if (t >= VDF && isFFN) asm volatile("s_waitcnt vmcnt(%0)" :: "n"(VDF - 1) : "memory");
        V[t] = __builtin_amdgcn_raw_buffer_load_b128(rv, vo, ((t + rot) & 15) * 8192, 2);
        __builtin_amdgcn_sched_barrier(0);
      }
    }
    __builtin_amdgcn_s_setprio(3);
    lds_add(SY + C_VISS, lane);
    STAMP(2);
    lds_wait_ge(SY + F_P + u, 1);
    f4 acc[8];
#pragma unroll
    for (int G = 0; G < 2; G++) {
      const v4u pf = *(LAS v4u*)(L + L_PFR + ((u * 2 + G) * 16 + g * 4 + (c & 3)) * 16);
#pragma unroll
      for (int d2 = 0; d2 < 4; d2++) {
        v4u lo, hi;
#pragma unroll
        for (int t2 = 0; t2 < 4; t2++) {
          const u32 va = V[8 * G + 2 * t2][d2], vb = V[8 * G + 2 * t2 + 1][d2];
          lo[t2] = __builtin_amdgcn_perm(vb, va, 0x05040100u);
          hi[t2] = __builtin_amdgcn_perm(vb, va, 0x07060302u);
        }
        if (G == 0) {
          acc[2 * d2] = mfma16(pf, lo, f4{0.f, 0.f, 0.f, 0.f});
          acc[2 * d2 + 1] = mfma16(pf, hi, f4{0.f, 0.f, 0.f, 0.f});
        } else {
          acc[2 * d2] = mfma16(pf, lo, acc[2 * d2]);
          acc[2 * d2 + 1] = mfma16(pf, hi, acc[2 * d2 + 1]);
        }
      }
      __builtin_amdgcn_sched_barrier(0);
    }
    STAMP(3);
    if (lane < 16) {
#pragma unroll
      for (int dd = 0; dd < 8; dd++) *(LAS f4*)(L + L_MRG + u * 2048 + dd * 256 + c * 16) = acc[dd];
    }
    lds_add(SY + C_O, lane);
    if (u < 2) {
      lds_wait_ge(SY + F_FAC, 1);
      f4 fac[8];
#pragma unroll
      for (int uu = 0; uu < 8; uu++) fac[uu] = *(LAS f4*)(L + L_FAC + uu * 16);
      lds_spin_ge(SY + C_O, 8);
      STAMP(20);
      f4 sacc = *(LAS f4*)(L + L_MRG + (u * 64 + lane) * 16) * fac[0];
#pragma unroll
      for (int uu = 1; uu < 8; uu++) sacc += *(LAS f4*)(L + L_MRG + uu * 2048 + (u * 64 + lane) * 16) * fac[uu];
      __builtin_amdgcn_raw_buffer_store_b128(canon4(sacc), rs, WS_MB1 + slot * 2048 + (u * 64 + lane) * 16, 0, AUX_SC1);
      lds_add(SY + C_PUB, lane);
      STAMP(4);
    }
    if (isWO) {
      const int bq = u >> 2;
      const int off = WS_MB1 + ((bq * 8 + x8) * 16 + g) * 2048 + (wi & 1) * 1024 + (u & 3) * 256 + c * 16;
      lds_wait_ge(SY + F_WT, 1);
      f4 wt[4];
#pragma unroll
      for (int i = 0; i < 4; i++) wt[i] = *(LAS f4*)(L + L_WT + (bq * 16 + g + 4 * i) * 16);
      v4u a0, a1, a2, a3;
      STAMP(31);
      poll4(rs, off, off + 4 * 2048, off + 8 * 2048, off + 12 * 2048, a0, a1, a2, a3);
      const float o = rscatter((F4(a0) * wt[0] + F4(a1) * wt[1]) + (F4(a2) * wt[2] + F4(a3) * wt[3]));
      *(LAS u16*)(L + L_OB + bq * 512 + ((u & 3) * 64 + c * 4 + g) * 2) = (u16)cvtpk(o, o);
      lds_add(SY + C_H, lane);
      STAMP(5);
      if (u == 0 && j < 8) {
        const int fi = x8 * 8 + j, bb = fi >> 5, tile = fi & 31;
        const int q4 = lane & 3, p16 = lane >> 2;
        const u32 xv = *(const GAS u16*)(a.x + bb * DM + tile * 16 + q4 * 4 + g);
        const int o2 = WS_MB2 + ((bb * 16 + p16) * 512 + tile * 16 + q4 * 4) * 4;
        v4u at;
        _Pragma("clang loop unroll(disable)") for (int spins = 0; spins < SPIN_CAP; ++spins) {
          CBAR();
          at = __builtin_amdgcn_raw_buffer_load_b128(rs, o2, 0, AUX_SC1);
          if (__all(valid4(at))) break;
        }
        float t = rscatter(F4(at));
        t += dpp<ROR4>(t);
        t += dpp<XOR8>(t);
        const float hv = rbf(bflo(xv) + rbf(t));
        const int o3 = WS_MB3 + (bb * 32 + tile) * 56 * 64 + lane * 16;
        const bool has3 = lane < 32;
        poll4(rs, o3, o3 + 1024, o3 + 2048, has3 ? o3 + 3072 : o3, a0, a1, a2, a3);
        f4 ds = (F4(a0) + F4(a1)) + F4(a2);
        if (has3) ds += F4(a3);
        t = rscatter(ds);
        t += dpp<ROR4>(t);
        t += dpp<XOR8>(t);
        const float ov = hv + rbf(t);
        if (c < 4) *(GAS u16*)(a.out + bb * DM + tile * 16 + q4 * 4 + g) = (u16)cvtpk(ov, ov);
        STAMP(9);
      }
    } else if (isFFN) {
      const u32 xv = *(const GAS u16*)(a.x + fb * DM + u * 64 + c * 4 + g);
      const int off = WS_MB2 + ((fb * 16 + g) * 512 + u * 64 + c * 4) * 4;
      lds_wait_ge(SY + F_FL, 1);
      lds_wait_ge(SY + C_W, 4);
      v4u bd[4];
      {
        const int r0_ = 8 * g + (c >> 2);
        const int t0_ = (r0_ & 3) | ((g & 1) << 2);
#pragma unroll
        for (int tt = 0; tt < 4; tt++) {
          const int ct = u * 4 + tt;
          v2u b0 = trread(L + L_DN + r0_ * 1024 + ((ct ^ t0_) * 32) + (c & 3) * 8);
          v2u b1 = trread(L + L_DN + (r0_ + 4) * 1024 + ((ct ^ t0_) * 32) + (c & 3) * 8);
          bd[tt] = v4u{b0.x, b0.y, b1.x, b1.y};
        }
      }
      v4u a0, a1, a2, a3;
      STAMP(30);
      poll4(rs, off, off + 4 * 2048, off + 8 * 2048, off + 12 * 2048, a0, a1, a2, a3);
      const float at = rscatter((F4(a0) + F4(a1)) + (F4(a2) + F4(a3)));
      const float hs = bflo(xv) + rbf(at);
      *(LAS u16*)(L + L_OB + (u * 64 + c * 4 + g) * 2) = (u16)cvtpk(hs, hs);
      lds_add(SY + C_H, lane);
      STAMP(6);
      lds_spin_ge(SY + C_A, 4);
      const v4u af = *(LAS v4u*)(L + L_ACT + g * 16);
      const int o3 = c == 0 ? WS_MB3 + ((fb * 32 + u * 4) * 56 + ff) * 64 + g * 16 : OOB;
#pragma unroll
      for (int tt = 0; tt < 4; tt++) {
        const f4 d = mfma16(bd[tt], af, f4{0.f, 0.f, 0.f, 0.f});
        __builtin_amdgcn_raw_buffer_store_b128(canon4(d), rs, o3, tt * 56 * 64, AUX_SC1);
      }
      STAMP(8);
    }
  }
  OPAQUE(wv);
  if (wv < 8) {
    const int u = wv;
    lds_wait_ge(SY + C_Q, 4);
    STAMP(11);
    {
      v4u qf[4];
#pragma unroll
      for (int kk = 0; kk < 4; kk++) qf[kk] = *(LAS v4u*)(L + L_QS + (c & 3) * 256 + (32 * kk + 8 * g) * 2);
      float sc[16];
#pragma unroll
      for (int T = 0; T < 4; T++) {
        f4 d = {0.f, 0.f, 0.f, 0.f};
        const int row = 16 * T + 4 * (c & 3) + (c >> 2);
#pragma unroll
        for (int kk = 0; kk < 4; kk++) {
          const v4u kf = *(LAS v4u*)(L + L_KIMG + (64 * u + row) * 256 + (((4 * kk + g) ^ c) * 16));
          d = mfma16(kf, qf[kk], d);
        }
#pragma unroll
        for (int i = 0; i < 4; i++) sc[4 * T + i] = rbf(rbf(d[i]) * 0.08838834764831845f);
      }
      float m = sc[0];
#pragma unroll
      for (int i = 1; i < 16; i++) m = fmaxf(m, sc[i]);
      m = x16_max(m);
      m = x32_max(m);
      float l = 0.f;
#pragma unroll
      for (int i = 0; i < 16; i++) { sc[i] = __builtin_amdgcn_exp2f((sc[i] - m) * 1.4426950408889634f); l += sc[i]; }
      l = x16_sum(l);
      l = x32_sum(l);
      if (c < 4) {
#pragma unroll
        for (int G = 0; G < 2; G++) {
          v4u pf = {cvtpk(sc[8 * G], sc[8 * G + 1]), cvtpk(sc[8 * G + 2], sc[8 * G + 3]), cvtpk(sc[8 * G + 4], sc[8 * G + 5]), cvtpk(sc[8 * G + 6], sc[8 * G + 7])};
          *(LAS v4u*)(L + L_PFR + ((u * 2 + G) * 16 + g * 4 + c) * 16) = pf;
        }
        if (g == 0) *(LAS f2*)(L + L_ML + (u * 4 + c) * 8) = f2{m, l};
      }
      lds_set(SY + F_P + u, lane);
      lds_add(SY + C_ML, lane);
    }
    STAMP(13);
    if (u == 1) {
      lds_wait_ge(SY + C_ML, 8);
      const int h = lane & 3, uu = (lane >> 2) & 7;
      const f2 ml = *(LAS f2*)(L + L_ML + (uu * 4 + h) * 8);
      float mw = ml[0];
      mw = fmaxf(mw, x4_get(mw)); mw = fmaxf(mw, dpp<XOR8>(mw)); mw = x16_max(mw);
      const float fac = __builtin_amdgcn_exp2f((ml[0] - mw) * 1.4426950408889634f);
      float lw = ml[1] * fac;
      lw += x4_get(lw); lw += dpp<XOR8>(lw); lw = x16_sum(lw);
      if (lane < 4) __builtin_amdgcn_raw_buffer_store_b64(v2u{canon(mw), canon(lw)}, rs, WS_ST + slot * 32 + lane * 8, 0, AUX_SC1);
      if (lane < 32) *(LAS float*)(L + L_FAC + lane * 4) = fac;
      lds_set(SY + F_FAC, lane);
      STAMP(14);
    }
    if (u == 0) {
      const u64 E = *(LAS u64*)(SY + W_E);
      const u32 elo = (u32)(E + 1), ehi = (u32)((E + 1) >> 32);
      _Pragma("clang loop unroll(disable)") for (int spins = 0; spins < SPIN_CAP; ++spins) {
        CBAR();
        const v4u f0 = __builtin_amdgcn_raw_buffer_load_b128(rs, WS_FLAGS + lane * 16, 0, AUX_SC1);
        const v4u f1 = __builtin_amdgcn_raw_buffer_load_b128(rs, WS_FLAGS + 1024 + lane * 16, 0, AUX_SC1);
        const bool ok = (f0.x == elo) & (f0.y == ehi) & (f0.z == elo) & (f0.w == ehi) & (f1.x == elo) & (f1.y == ehi) & (f1.z == elo) & (f1.w == ehi);
        if (__all(ok)) break;
      }
      if (w == 0 && lane == 0) __hip_atomic_store((GAS u64*)(a.ws + WS_EPOCH), E + 1, __ATOMIC_RELAXED, __HIP_MEMORY_SCOPE_AGENT);
      lds_set(SY + F_FL, lane);
      STAMP(17);
    }
    if (isWO) {
      if (u == 0) {
        v2u st0, st1;
        if (STWAIT) lds_wait_ge(SY + C_VISS, 8);
        const int soff = WS_ST + (x8 * 16) * 32 + lane * 8;
        _Pragma("clang loop unroll(disable)") for (int spins = 0; spins < SPIN_CAP; ++spins) {
          CBAR();
          st0 = __builtin_amdgcn_raw_buffer_load_b64(rs, soff, 0, AUX_SC1);
          st1 = __builtin_amdgcn_raw_buffer_load_b64(rs, soff + 128 * 32, 0, AUX_SC1);
          if (__all((st0.x != SENT) & (st0.y != SENT) & (st1.x != SENT) & (st1.y != SENT))) break;
          __builtin_amdgcn_s_sleep(STSLEEP);
        }
#pragma unroll
        for (int bb = 0; bb < 2; bb++) {
          const float ms = __uint_as_float(bb ? st1.x : st0.x), ls = __uint_as_float(bb ? st1.y : st0.y);
          float M = ms;
          M = fmaxf(M, x4_get(M)); M = fmaxf(M, dpp<XOR8>(M)); M = x16_max(M); M = x32_max(M);
          const float ex = __builtin_amdgcn_exp2f((ms - M) * 1.4426950408889634f);
          float Ls = ls * ex;
          Ls += x4_get(Ls); Ls += dpp<XOR8>(Ls); Ls = x16_sum(Ls); Ls = x32_sum(Ls);
          *(LAS float*)(L + L_WT + bb * 256 + lane * 4) = ex / Ls;
        }
        lds_set(SY + F_WT, lane);
        STAMP(18);
      }
      if (u >= 4) {
        lds_wait_ge(SY + C_ML, 8);
        if (WLATE & 1) lds_wait_ge(SY + C_PUB, 2); else lds_wait_ge(SY + C_VISS, 8);
        __builtin_amdgcn_s_setprio(0);
        const __amdgpu_buffer_rsrc_t rw = mkrsrc(a.wo, 4096 * DM * 2);
        const u32 vo = (u32)(((r8 & 3) * 128 + 8 * (r8 >> 2)) * 1024 + wjj * 128);
        dma_run2<1>(rw, vo + swA, vo + swB, (u32)((x8 * 512 + 4 * (wi & 1) + (u - 4)) * 1024), 16 * 1024, (u32)(size_t)(L + L_WO) + (u - 4) * 8192, 4);
        VM0();
        lds_add(SY + C_W, lane);
        STAMP(15);
      } else {
        lds_wait_ge(SY + C_W, 4);
        v4u bf[8];
        const int r0_ = 8 * g + (c >> 2);
        const int sg0 = ((r0_ >> 1) & 1) | ((g & 1) << 1);
        LAS char* bp = L + L_WO + r0_ * 128 + ((u ^ sg0) * 32) + (c & 3) * 8;
#pragma unroll
        for (int ks = 0; ks < 8; ks++) {
          v2u b0 = trread(bp + ks * 4096), b1 = trread(bp + ks * 4096 + 512);
          bf[ks] = v4u{b0.x, b0.y, b1.x, b1.y};
        }
        STAMP(19);
        lds_spin_ge(SY + C_H, 8);
        STAMP(21);
        f4 d = {0.f, 0.f, 0.f, 0.f};
#pragma unroll
        for (int ks = 0; ks < 8; ks++) {
          const v4u of = *(LAS v4u*)(L + L_OB + (c & 1) * 512 + (32 * ks + 8 * g) * 2);
          d = mfma16(bf[ks], of, d);
        }
        __builtin_amdgcn_raw_buffer_store_b128(canon4(d), rs, c < 2 ? WS_MB2 + ((c * 16 + wi) * 512 + wjj * 64 + u * 16 + g * 4) * 4 : OOB, 0, AUX_SC1);
        STAMP(16);
      }
    } else if (isFFN) {
      if (u >= 4) {
        lds_wait_ge(SY + C_ML, 8);
        if (WSTART) lds_wait_ge(SY + C_VISS, 8);
        __builtin_amdgcn_s_setprio(0);
        const int lam = u - 4;
        const __amdgpu_buffer_rsrc_t rg = mkrsrc(a.wg, DM * FF * 2), ru = mkrsrc(a.wu, DM * FF * 2), rd = mkrsrc(a.wd, FF * DM * 2);
        const int r16 = lane >> 2, q4 = lane & 3;
        const u32 vg = (u32)(r16 * FF * 2 + ff * 64 + ((q4 ^ (r16 >> 3)) * 16)), vu = (u32)(r16 * FF * 2 + ff * 64 + ((q4 ^ ((r16 >> 3) | 2)) * 16));
        u32 mode = 0;
#define PACE() do { if (mode == 0) { asm volatile("s_waitcnt vmcnt(%0)" :: "n"(WD1 - 1) : "memory"); \
                      mode = __builtin_amdgcn_readfirstlane(__hip_atomic_load(SY + C_PUB, __ATOMIC_RELAXED, __HIP_MEMORY_SCOPE_WORKGROUP)) >= 2; } } while (0)
        _Pragma("clang loop unroll(disable)") for (int t = 0; t < 8; t++) {
          const u32 P = lam * 8 + t;
          PACE();
          bl_lds(rg, vg, P * (16 * FF * 2), (u32)(size_t)(L + L_GU) + P * 1024);
          PACE();
          bl_lds(ru, vu, P * (16 * FF * 2), (u32)(size_t)(L + L_GU) + 32768 + P * 1024);
        }
        _Pragma("clang loop unroll(disable)") for (int t = 0; t < 8; t++) {
          const u32 k = lam * 8 + t;
          const u32 tau = (k & 3) | (((k >> 3) & 1) << 2);
          PACE();
          bl_lds(rd, (u32)((((lane >> 1) ^ tau) * 2 + (lane & 1)) * 16), (ff * 32 + k) * 1024, (u32)(size_t)(L + L_DN) + k * 1024);
        }
        VM0();
        lds_add(SY + C_W, lane);
        STAMP(15);
      } else {
        lds_wait_ge(SY + C_W, 4);
        v4u bfr[16];
        const int mu = (c >> 1) & 1;
        LAS char* bp = L + L_GU + mu * 32768 + (8 * g + (c >> 2)) * 64 + ((u ^ ((g & 1) | (mu << 1))) * 16) + (c & 1) * 8;
        f4 cs = {0.f, 0.f, 0.f, 0.f};
        const v4u ones = {0x3F803F80u, 0x3F803F80u, 0x3F803F80u, 0x3F803F80u};
#pragma unroll
        for (int ks = 0; ks < 16; ks++) {
          v2u b0 = trread(bp + ks * 2048), b1 = trread(bp + ks * 2048 + 256);
          bfr[ks] = v4u{b0.x, b0.y, b1.x, b1.y};
          cs = mfma16(ones, bfr[ks], cs);
        }
        const float colsum = cs[0];
        STAMP(23);
        lds_spin_ge(SY + C_H, 8);
        STAMP(24);
        const v4u hv = *(LAS v4u*)(L + L_OB + lane * 16);
        f4 d = {0.f, 0.f, 0.f, 0.f};
#pragma unroll
        for (int ks = 0; ks < 16; ks++) {
          const v4u af = *(LAS v4u*)(L + L_OB + (32 * ks + 8 * g) * 2);
          d = mfma16(af, bfr[ks], d);
        }
        float sm = 0.f;
#pragma unroll
        for (int e = 0; e < 4; e++) sm += bflo(hv[e]) + bfhi(hv[e]);
        const float mean = wave_sum(sm) * (1.0f / DM);
        float q2 = 0.f;
#pragma unroll
        for (int e = 0; e < 4; e++) {
          float t = bflo(hv[e]) - mean; q2 = fmaf(t, t, q2);
          t = bfhi(hv[e]) - mean; q2 = fmaf(t, t, q2);
        }
        q2 = wave_sum(q2);
        const float m = rbf(mean), vv = rbf(q2 * (1.0f / DM));
        const float den = rbf(sqrtf(rbf(vv + 1e-5f)));
        const float pre = rbf((d[0] - m * colsum) / den);
        const float other = dpp<XOR8>(pre);
        const float sg = rbf(pre / (1.0f + __expf(-pre)));
        const float act = sg * other;
        if (lane < 8) *(LAS u16*)(L + L_ACT + (u * 8 + lane) * 2) = (u16)cvtpk(act, act);
        lds_add(SY + C_A, lane);
        STAMP(7);
      }
    }
  }
  FLUSH_TS();
}

extern "C" void kernel_launch(void* const* d_in, const int* in_sizes, int n_in, void* d_out, int out_size,
                              void* d_ws, size_t ws_size, hipStream_t stream) {
  Args a;
  a.ws = (char*)d_ws;
  a.x = (const u16*)d_in[0];
  a.k = (const u16*)d_in[1];
  a.v = (const u16*)d_in[2];
  a.wq = (const u16*)d_in[3];
  a.wo = (const u16*)d_in[4];
  a.wg = (const u16*)d_in[6];
  a.wu = (const u16*)d_in[7];
  a.wd = (const u16*)d_in[8];
  a.out = (u16*)d_out;
  hipLaunchKernelGGL(fused_block, dim3(NWG), dim3(NT), 0, stream, a);
}
```

```cpp
#include <hip/hip_runtime.h>
#include <stdint.h>

typedef unsigned short u16;
typedef unsigned int u32;
typedef unsigned long long u64;
typedef u32 v4u __attribute__((ext_vector_type(4)));
typedef u32 v2u __attribute__((ext_vector_type(2)));
typedef float f4 __attribute__((ext_vector_type(4)));
typedef float f2 __attribute__((ext_vector_type(2)));
typedef short s4v __attribute__((ext_vector_type(4)));
typedef __bf16 bf8 __attribute__((ext_vector_type(8)));
typedef __bf16 bf2 __attribute__((ext_vector_type(2)));
#define GAS __attribute__((address_space(1)))
#define LAS __attribute__((address_space(3)))

#define DEBUG_TS 0
#define KD 8
#define VD 4
#define QSLEEP 40
#define WLATE 0
#define STSLEEP 8
#define STWAIT 1
#define WD1 3
#define KDF 16
#define VDF 8
#define KSLOW 2
#define WSTART 1
#define PSLEEP 0
#define ROTF 0
#define QSPREAD 1
#define QFAKE 4
#define PRET 0
#define KPOL "nt"
#define VAUX 2
#define CODE_KB 18

constexpr int DM = 512, DH = 128, FF = 1792;
constexpr int NWG = 256, NT = 1024;
constexpr int WS_EPOCH = 0;
constexpr int WS_FLAGS = 4096;
constexpr int WS_QG = 8192;
constexpr int WS_ST = WS_QG + 2 * 4 * 4096 * 8;
constexpr int WS_MB1 = WS_ST + 256 * 32;
constexpr int WS_MB2 = WS_MB1 + 256 * 2048;
constexpr int WS_MB3 = WS_MB2 + 2 * 16 * 2048;
constexpr int WS_QG2 = 2 << 20;
constexpr int WS_END = WS_QG2 + 9 * 262144;
constexpr int WS_DBG = (WS_END + 4095) / 4096 * 4096;
#define QADDR(off) (WS_QG2 + (off) + (((off) >> 12) & 7) * 262144)
constexpr u32 SENT = 0xFFFFFFFFu;
constexpr int AUX_SC1 = 16;
constexpr int OOB = 0x7FFFFFF0;
constexpr int SPIN_CAP = 1 << 16;
constexpr int L_KIMG = 0;
constexpr int L_WO = 0;
constexpr int L_GU = 0;
constexpr int L_DN = 65536;
constexpr int L_WQ = 131072;
constexpr int L_MRG = L_WQ;
constexpr int L_PFR = L_WQ + 16384;
constexpr int L_QPART = L_PFR + 4096;
constexpr int L_QS = L_QPART + 2048;
constexpr int L_OB = L_QS + 1024;
constexpr int L_ML = L_OB + 1024;
constexpr int L_FAC = L_ML + 256;
constexpr int L_ACT = L_FAC + 128;
constexpr int L_WT = L_ACT + 64;
constexpr int L_SYNC = L_WT + 512;
constexpr int L_DBG = L_SYNC + 128;
constexpr int L_DUMMY = L_DBG + 256;
constexpr int LDS_BYTES = L_DUMMY + 1024;
static_assert(LDS_BYTES <= 163840, "lds");
enum { C_QP = 0, C_Q = 1, C_ML = 2, F_FAC = 3, C_VISS = 4, C_W = 5, F_E = 6, F_FL = 7, F_P = 8  , W_E = 16  ,
       C_PUB = 18, C_O = 19, F_WT = 20, C_H = 21, C_A = 22, F_K = 24   };

struct Args {
  char* ws;
  const u16 *x, *wq, *k, *v, *wo, *wg, *wu, *wd;
  u16* out;
};

__device__ __forceinline__ float bflo(u32 v) { return __uint_as_float(v << 16); }
__device__ __forceinline__ float bfhi(u32 v) { return __uint_as_float(v & 0xFFFF0000u); }
__device__ __forceinline__ u32 cvtpk(float lo, float hi) {
  return __builtin_bit_cast(u32, __builtin_convertvector(f2{lo, hi}, bf2));
}
__device__ __forceinline__ float rbf(float f) { return bflo(cvtpk(f, f)); }
template <int CTRL> __device__ __forceinline__ float dpp(float x) {
  return __builtin_bit_cast(float, __builtin_amdgcn_mov_dpp(__builtin_bit_cast(int, x), CTRL, 0xf, 0xf, true));
}
constexpr int XOR1 = 0xB1, XOR2 = 0x4E, XOR7 = 0x141, XOR8 = 0x128, ROR4 = 0x124;
__device__ __forceinline__ float x16_sum(float x) {
  auto s = __builtin_amdgcn_permlane16_swap(__float_as_uint(x), __float_as_uint(x), false, false);
  return __uint_as_float(s[0]) + __uint_as_float(s[1]);
}
__device__ __forceinline__ float x32_sum(float x) {
  auto s = __builtin_amdgcn_permlane32_swap(__float_as_uint(x), __float_as_uint(x), false, false);
  return __uint_as_float(s[0]) + __uint_as_float(s[1]);
}
__device__ __forceinline__ float x16_max(float x) {
  auto s = __builtin_amdgcn_permlane16_swap(__float_as_uint(x), __float_as_uint(x), false, false);
  return fmaxf(__uint_as_float(s[0]), __uint_as_float(s[1]));
}
__device__ __forceinline__ float x32_max(float x) {
  auto s = __builtin_amdgcn_permlane32_swap(__float_as_uint(x), __float_as_uint(x), false, false);
  return fmaxf(__uint_as_float(s[0]), __uint_as_float(s[1]));
}
__device__ __forceinline__ float x4_get(float x) { return dpp<XOR7>(dpp<0x1B>(x)); }
__device__ __forceinline__ float wave_sum(float s) {
  s += dpp<XOR1>(s); s += dpp<XOR2>(s); s += dpp<XOR7>(s); s += dpp<XOR8>(s);
  s = x16_sum(s);
  return x32_sum(s);
}
__device__ __forceinline__ float rscatter(f4 v) {
  auto p = __builtin_amdgcn_permlane32_swap(__float_as_uint(v[0]), __float_as_uint(v[2]), false, false);
  auto q = __builtin_amdgcn_permlane32_swap(__float_as_uint(v[1]), __float_as_uint(v[3]), false, false);
  const float ps = __uint_as_float(p[0]) + __uint_as_float(p[1]);
  const float qs = __uint_as_float(q[0]) + __uint_as_float(q[1]);
  auto r = __builtin_amdgcn_permlane16_swap(__float_as_uint(ps), __float_as_uint(qs), false, false);
  return __uint_as_float(r[0]) + __uint_as_float(r[1]);
}
__device__ __forceinline__ f4 mfma16(v4u a, v4u b, f4 c) {
  return __builtin_amdgcn_mfma_f32_16x16x32_bf16(__builtin_bit_cast(bf8, a), __builtin_bit_cast(bf8, b), c, 0, 0, 0);
}
__device__ __forceinline__ v2u trread(LAS char* p) {
  s4v r = __builtin_amdgcn_ds_read_tr16_b64_v4i16((LAS s4v*)p);
  return __builtin_bit_cast(v2u, r);
}
__device__ __forceinline__ u32 canon(float f) {
  u32 u = __float_as_uint(f);
  return u == SENT ? 0x7FC00000u : u;
}
__device__ __forceinline__ v4u canon4(f4 v) { return v4u{canon(v[0]), canon(v[1]), canon(v[2]), canon(v[3])}; }
__device__ __forceinline__ bool valid4(v4u v) { return (v.x != SENT) & (v.y != SENT) & (v.z != SENT) & (v.w != SENT); }
#define LGKM0() asm volatile("s_waitcnt lgkmcnt(0)" ::: "memory")
#define VM0() asm volatile("s_waitcnt vmcnt(0)" ::: "memory")
#define CBAR() asm volatile("" ::: "memory")
#define OPAQUE(x) asm volatile("" : "+s"(x))
__device__ __forceinline__ void wg_barrier() {
  __builtin_amdgcn_sched_barrier(0);
  LGKM0();
  __builtin_amdgcn_s_barrier();
  CBAR();
  __builtin_amdgcn_sched_barrier(0);
}
__device__ __forceinline__ void lds_wait_ge(LAS u32* p, u32 want) {
  _Pragma("clang loop unroll(disable)") for (int spins = 0; spins < SPIN_CAP; ++spins) {
    u32 v = __hip_atomic_load(p, __ATOMIC_RELAXED, __HIP_MEMORY_SCOPE_WORKGROUP);
    if (__builtin_amdgcn_readfirstlane(v) >= want) break;
    __builtin_amdgcn_s_sleep(1);
  }
  CBAR();
}
__device__ __forceinline__ void lds_spin_ge(LAS u32* p, u32 want) {
  _Pragma("clang loop unroll(disable)") for (int spins = 0; spins < SPIN_CAP * 16; ++spins) {
    u32 v = __hip_atomic_load(p, __ATOMIC_RELAXED, __HIP_MEMORY_SCOPE_WORKGROUP);
    if (__builtin_amdgcn_readfirstlane(v) >= want) break;
  }
  CBAR();
}
__device__ __forceinline__ void lds_add(LAS u32* p, int lane) {
  LGKM0();
  if (lane == 0) __hip_atomic_fetch_add(p, 1u, __ATOMIC_RELAXED, __HIP_MEMORY_SCOPE_WORKGROUP);
}
__device__ __forceinline__ void lds_set(LAS u32* p, int lane) {
  LGKM0();
  if (lane == 0) __hip_atomic_store(p, 1u, __ATOMIC_RELAXED, __HIP_MEMORY_SCOPE_WORKGROUP);
}
template <int NTP> __device__ __forceinline__ void glds16(const void* gsrc, u32 lds_dst) {
  u32 keep;
  if (NTP)
    asm volatile("s_mov_b32 %0, m0\n\ts_mov_b32 m0, %2\n\ts_nop 0\n\tglobal_load_lds_dwordx4 %1, off nt\n\ts_mov_b32 m0, %0"
                 : "=&s"(keep) : "v"(gsrc), "s"(lds_dst) : "memory");
  else
    asm volatile("s_mov_b32 %0, m0\n\ts_mov_b32 m0, %2\n\ts_nop 0\n\tglobal_load_lds_dwordx4 %1, off\n\ts_mov_b32 m0, %0"
                 : "=&s"(keep) : "v"(gsrc), "s"(lds_dst) : "memory");
}
template <int NTP> __device__ __forceinline__ void dma_run2(__amdgpu_buffer_rsrc_t r, u32 va, u32 vb, u32 so, u32 gstep, u32 lds0, u32 npairs) {
  u32 keep;
  if (NTP)
    asm volatile(
        "s_nop 4\n\ts_mov_b32 %[keep], m0\n\ts_mov_b32 m0, %[l0]\n"
        "1:\n\ts_sub_u32 %[cnt], %[cnt], 1\n\t"
        "buffer_load_dwordx4 %[va], %[r], %[so] offen nt lds\n\t"
        "s_add_u32 m0, m0, 0x400\n\ts_add_u32 %[so], %[so], %[gs]\n\t"
        "buffer_load_dwordx4 %[vb], %[r], %[so] offen nt lds\n\t"
        "s_add_u32 m0, m0, 0x400\n\ts_add_u32 %[so], %[so], %[gs]\n\t"
        "s_cmp_lg_u32 %[cnt], 0\n\ts_cbranch_scc1 1b\n\ts_mov_b32 m0, %[keep]"
        : [keep] "=&s"(keep), [so] "+s"(so), [cnt] "+s"(npairs)
        : [va] "v"(va), [vb] "v"(vb), [r] "s"(r), [l0] "s"(lds0), [gs] "s"(gstep) : "memory", "scc");
  else
    asm volatile(
        "s_nop 4\n\ts_mov_b32 %[keep], m0\n\ts_mov_b32 m0, %[l0]\n"
        "1:\n\ts_sub_u32 %[cnt], %[cnt], 1\n\t"
        "buffer_load_dwordx4 %[va], %[r], %[so] offen lds\n\t"
        "s_add_u32 m0, m0, 0x400\n\ts_add_u32 %[so], %[so], %[gs]\n\t"
        "buffer_load_dwordx4 %[vb], %[r], %[so] offen lds\n\t"
        "s_add_u32 m0, m0, 0x400\n\ts_add_u32 %[so], %[so], %[gs]\n\t"
        "s_cmp_lg_u32 %[cnt], 0\n\ts_cbranch_scc1 1b\n\ts_mov_b32 m0, %[keep]"
        : [keep] "=&s"(keep), [so] "+s"(so), [cnt] "+s"(npairs)
        : [va] "v"(va), [vb] "v"(vb), [r] "s"(r), [l0] "s"(lds0), [gs] "s"(gstep) : "memory", "scc");
}
__device__ __forceinline__ void bl_lds(__amdgpu_buffer_rsrc_t r, u32 vo, u32 so, u32 lds) {
  u32 keep;
  asm volatile("s_mov_b32 %[keep], m0\n\ts_mov_b32 m0, %[l]\n\ts_nop 0\n\tbuffer_load_dwordx4 %[v], %[r], %[so] offen lds\n\ts_mov_b32 m0, %[keep]"
               : [keep] "=&s"(keep) : [v] "v"(vo), [r] "s"(r), [so] "s"(so), [l] "s"(lds) : "memory");
}
__device__ __forceinline__ __amdgpu_buffer_rsrc_t mkrsrc(const void* p, int bytes) {
  return __builtin_amdgcn_make_buffer_rsrc((void*)p, 0, bytes, 0x00020000);
}

#define STAMP(idx) do {} while (0)
#define FLUSH_TS() do {} while (0)

__device__ __forceinline__ void poll4(__amdgpu_buffer_rsrc_t rs, int o0, int o1, int o2, int o3, v4u& a0, v4u& a1, v4u& a2, v4u& a3) {
  a0 = __builtin_amdgcn_raw_buffer_load_b128(rs, o0, 0, AUX_SC1);
  a1 = __builtin_amdgcn_raw_buffer_load_b128(rs, o1, 0, AUX_SC1);
  a2 = __builtin_amdgcn_raw_buffer_load_b128(rs, o2, 0, AUX_SC1);
  a3 = __builtin_amdgcn_raw_buffer_load_b128(rs, o3, 0, AUX_SC1);
  _Pragma("clang loop unroll(disable)") for (int spins = 0; spins < SPIN_CAP; ++spins) {
    const bool v0 = valid4(a0), v1 = valid4(a1), v2 = valid4(a2), v3 = valid4(a3);
    if (__all(v0 && v1 && v2 && v3)) break;
    CBAR();
    if (PSLEEP) __builtin_amdgcn_s_sleep(PSLEEP);
    const v4u b0 = __builtin_amdgcn_raw_buffer_load_b128(rs, v0 ? OOB : o0, 0, AUX_SC1);
    const v4u b1 = __builtin_amdgcn_raw_buffer_load_b128(rs, v1 ? OOB : o1, 0, AUX_SC1);
    const v4u b2 = __builtin_amdgcn_raw_buffer_load_b128(rs, v2 ? OOB : o2, 0, AUX_SC1);
    const v4u b3 = __builtin_amdgcn_raw_buffer_load_b128(rs, v3 ? OOB : o3, 0, AUX_SC1);
    a0 = v0 ? a0 : b0; a1 = v1 ? a1 : b1; a2 = v2 ? a2 : b2; a3 = v3 ? a3 : b3;
  }
}
#define F4(x) __builtin_bit_cast(f4, x)

__global__ void __launch_bounds__(NT) fused_block(Args a) {
  __shared__ __attribute__((aligned(16))) char smem[LDS_BYTES];
  LAS char* L = (LAS char*)smem;
  LAS u32* SY = (LAS u32*)(L + L_SYNC);
  u64 pc0;
  asm volatile("s_getpc_b64 %0" : "=s"(pc0));
  const int tid = threadIdx.x, lane = tid & 63, wave = __builtin_amdgcn_readfirstlane(tid >> 6);
  const int w = blockIdx.x, x8 = w & 7, j = w >> 3;
  const int slot = x8 * 32 + j;
  const int g = lane >> 4, c = lane & 15;
  const int r8 = lane >> 3, ch = lane & 7;
  const __amdgpu_buffer_rsrc_t rs = mkrsrc(a.ws, WS_END);
  if (tid < 32) SY[tid] = 0;
  if (wave == 0) STAMP(0);
  int wv = wave;
  const u32 swA = (u32)((((ch >> 1) ^ ((r8 >> 1) & 1)) * 2 + (ch & 1)) * 16);
  const u32 swB = (u32)((((ch >> 1) ^ (((r8 >> 1) & 1) | 2)) * 2 + (ch & 1)) * 16);
  const u32 rot = ROTF;
  const int cb = w & 63, kq = w >> 6;
  v4u xs0, xs1, xf;
  if (wv < 4) {
    __builtin_amdgcn_s_setprio(3);
    const __amdgpu_buffer_rsrc_t rq = mkrsrc(a.wq, DM * 4096 * 2);
    const u32 vo = (u32)(r8 * 8192 + cb * 128);
    dma_run2<1>(rq, vo + swA, vo + swB, (u32)((128 * kq + 32 * wv) * 8192), 8 * 8192, (u32)(size_t)(L + L_WQ) + wv * 4096, 2);
    xs0 = *(const GAS v4u*)(a.x + lane * 8);
    xs1 = *(const GAS v4u*)(a.x + DM + lane * 8);
    xf = *(const GAS v4u*)(a.x + (c & 1) * DM + 128 * kq + 32 * wv + 8 * g);
    __builtin_amdgcn_sched_barrier(0);
  }
  OPAQUE(wv);
  if (wv > 8) {
    const char* cp = (const char*)(pc0 & ~(u64)255) + min((j & 3) * 7 + (wv - 9), CODE_KB - 1) * 1024 + lane * 16;
    glds16<0>(cp, (u32)(size_t)(L + L_DUMMY));
    if ((PRET & 1) && wv == 15) bl_lds(rs, (u32)QADDR((((lane >> 5) * 4 + kq) * 4096 + cb * 64 + (lane & 31) * 2) * 8), 0, (u32)(size_t)(L + L_DUMMY));
  }
  const bool isWO = j < 16;
  const int wi = 2 * x8 + ((j >> 3) & 1), wjj = j & 7;
  const int fidx = j - 16;
  const bool isFFN = (j >= 16) && (x8 < 4 || fidx < 12);
  const int fblk = (x8 < 4 ? x8 * 4 : 4 + x8 * 3) + (fidx >> 2);
  const int ff = fblk * 2 + ((fidx >> 1) & 1);
  const int fb = fidx & 1;
  const int co0 = WS_MB1 + slot * 2048 + lane * 16;
  const int o3_ = WS_MB3 + ((fb * 32 + (lane >> 2)) * 56 + ff) * 64 + (lane & 3) * 16;
  const int co2 = isFFN ? o3_ : (isWO && lane < 32 ? WS_MB2 + ((g * 16 + wi) * 512 + wjj * 64 + c * 4) * 4 : OOB);
  const int co3 = isFFN ? o3_ + 16 * 56 * 64 : OOB;
  const int co4 = lane < 2 ? WS_ST + slot * 32 + (lane & 1) * 16 : OOB;
#define CLEARS() do { const v4u sv = {SENT, SENT, SENT, SENT}; \
    __builtin_amdgcn_raw_buffer_store_b128(sv, rs, co0, 0, AUX_SC1); \
    __builtin_amdgcn_raw_buffer_store_b128(sv, rs, co0 + 1024, 0, AUX_SC1); \
    __builtin_amdgcn_raw_buffer_store_b128(sv, rs, co2, 0, AUX_SC1); \
    __builtin_amdgcn_raw_buffer_store_b128(sv, rs, co3, 0, AUX_SC1); \
    __builtin_amdgcn_raw_buffer_store_b128(sv, rs, co4, 0, AUX_SC1); } while (0)
  OPAQUE(wv);
  u64 E0 = 0;
  if (wv == 8) {
    __builtin_amdgcn_s_setprio(3);
    E0 = __hip_atomic_load((GAS u64*)(a.ws + WS_EPOCH), __ATOMIC_RELAXED, __HIP_MEMORY_SCOPE_AGENT);
    if (PRET & 2) {
      bl_lds(rs, (u32)co0, 0, (u32)(size_t)(L + L_DUMMY));
      bl_lds(rs, (u32)co0 + 1024, 0, (u32)(size_t)(L + L_DUMMY));
      bl_lds(rs, (u32)co2, 0, (u32)(size_t)(L + L_DUMMY));
      bl_lds(rs, (u32)co3, 0, (u32)(size_t)(L + L_DUMMY));
      bl_lds(rs, (u32)(lane == 2 ? WS_FLAGS + (w & ~1) * 8 : co4), 0, (u32)(size_t)(L + L_DUMMY));
    } else CLEARS();
    __builtin_amdgcn_sched_barrier(0);
  }
  wg_barrier();
  OPAQUE(wv);
  if (wv >= 4 && wv < 8) {
    __builtin_amdgcn_s_setprio(2);
    const int lam = wv - 4;
    const __amdgpu_buffer_rsrc_t rk = mkrsrc(a.k + (size_t)slot * 512 * DH, 131072);
    const u32 vo = (u32)(lam * 1024 + g * 256);
    const u32 v0 = vo + ((c ^ (4 * g + 0)) * 16), v1 = vo + ((c ^ (4 * g + 1)) * 16), v2 = vo + ((c ^ (4 * g + 2)) * 16), v3 = vo + ((c ^ (4 * g + 3)) * 16);
#define KPAIR(V) \
        "s_waitcnt vmcnt(%[kd])\n\t" \
        "buffer_load_dwordx4 " V ", %[rk], %[so] offen " KPOL " lds\n\t" \
        "s_add_u32 m0, m0, 0x10000\n\ts_add_u32 %[so], %[so], 0x1000\n\t" \
        "s_waitcnt vmcnt(%[kd])\n\t" \
        "buffer_load_dwordx4 " V ", %[rk], %[so] offen " KPOL " lds\n\t" \
        "s_sub_u32 m0, m0, 0xfc00\n\ts_add_u32 %[so], %[so], 0x1000\n\ts_and_b32 %[so], %[so], 0x1ffff\n\t"
#define KLOOP(DEPTH, IT0, NIT) do { u32 so = ((IT0) * 0x8000 + rot * 0x2000) & 0x1ffff, keep, cnt = NIT; \
    asm volatile( \
        "s_nop 4\n\ts_mov_b32 %[keep], m0\n\ts_mov_b32 m0, %[l0]\n" \
        "1:\n\ts_sub_u32 %[cnt], %[cnt], 1\n\t" \
        KPAIR("%[v0]") KPAIR("%[v1]") KPAIR("%[v2]") KPAIR("%[v3]") \
        "s_cmp_lg_u32 %[cnt], 0\n\ts_cbranch_scc1 1b\n\ts_mov_b32 m0, %[keep]" \
        : [keep] "=&s"(keep), [so] "+s"(so), [cnt] "+s"(cnt) \
        : [v0] "v"(v0), [v1] "v"(v1), [v2] "v"(v2), [v3] "v"(v3), [rk] "s"(rk), [l0] "s"((u32)(size_t)(L + L_KIMG) + (u32)lam * 16384u + (IT0) * 4096u), [kd] "n"((DEPTH) - 1) \
        : "memory", "scc"); } while (0)
    KLOOP(KD, 0, (isFFN ? KSLOW : 4));
    if (isFFN && KSLOW < 4) KLOOP(KDF, KSLOW, 4 - KSLOW);
    __builtin_amdgcn_s_setprio(3);
    if (lane == 0) __hip_atomic_store(SY + F_K + lam, 1u, __ATOMIC_RELAXED, __HIP_MEMORY_SCOPE_WORKGROUP);
    STAMP(10);
    asm volatile("s_waitcnt vmcnt(%0)" :: "n"(KD / 2) : "memory");
    if (lane == 0) __hip_atomic_store(SY + F_K + lam, 2u, __ATOMIC_RELAXED, __HIP_MEMORY_SCOPE_WORKGROUP);
    VM0();
    if (lane == 0) __hip_atomic_store(SY + F_K + lam, 3u, __ATOMIC_RELAXED, __HIP_MEMORY_SCOPE_WORKGROUP);
    STAMP(12);
  }
  OPAQUE(wv);
  if (wv < 4) {
    STAMP(25);
    float m0_, m1_, d0_, d1_;
    {
      float s0 = 0.f, s1 = 0.f;
#pragma unroll
      for (int e = 0; e < 4; e++) { s0 += bflo(xs0[e]) + bfhi(xs0[e]); s1 += bflo(xs1[e]) + bfhi(xs1[e]); }
      s0 = wave_sum(s0); s1 = wave_sum(s1);
      const float mean0 = s0 * (1.0f / DM), mean1 = s1 * (1.0f / DM);
      float q0 = 0.f, q1 = 0.f;
#pragma unroll
      for (int e = 0; e < 4; e++) {
        float t;
        t = bflo(xs0[e]) - mean0; q0 = fmaf(t, t, q0);
        t = bfhi(xs0[e]) - mean0; q0 = fmaf(t, t, q0);
        t = bflo(xs1[e]) - mean1; q1 = fmaf(t, t, q1);
        t = bfhi(xs1[e]) - mean1; q1 = fmaf(t, t, q1);
      }
      q0 = wave_sum(q0); q1 = wave_sum(q1);
      m0_ = mean0; m1_ = mean1; d0_ = q0; d1_ = q1;
    }
    const float mean = (c & 1) ? m1_ : m0_, qq = (c & 1) ? d1_ : d0_;
    const float m = rbf(mean), vv = rbf(qq * (1.0f / DM));
    const float den = rbf(sqrtf(rbf(vv + 1e-5f)));
    v4u af;
#pragma unroll
    for (int e = 0; e < 4; e++) af[e] = cvtpk(rbf(bflo(xf[e]) - m) / den, rbf(bfhi(xf[e]) - m) / den);
    STAMP(26);
    VM0();
    {
      const int r0_ = 8 * g + (c >> 2);
      const int sg0 = ((r0_ >> 1) & 1) | ((g & 1) << 1);
      LAS char* bp = L + L_WQ + wv * 4096 + r0_ * 128 + (c & 3) * 8;
      f4 d[4];
#pragma unroll
      for (int t = 0; t < 4; t++) {
        v2u b0 = trread(bp + ((t ^ sg0) * 32)), b1 = trread(bp + 512 + ((t ^ sg0) * 32));
        d[t] = mfma16(af, v4u{b0.x, b0.y, b1.x, b1.y}, f4{0.f, 0.f, 0.f, 0.f});
      }
      const f4 da = (g & 1) ? d[1] : d[0], db = (g & 1) ? d[3] : d[2];
      const f4 dd = (g & 2) ? db : da;
      *(LAS f2*)(L + L_QPART + wv * 512 + lane * 8) = f2{dd[0], dd[1]};
    }
    STAMP(27);
    lds_wait_ge(SY + F_E, 1);
    const u64 E = *(LAS u64*)(SY + W_E);
    const u32 tag = (u32)E + 1u;
    LGKM0();
    u32 cnt = 0;
    if (lane == 0) cnt = __hip_atomic_fetch_add(SY + C_QP, 1u, __ATOMIC_RELAXED, __HIP_MEMORY_SCOPE_WORKGROUP);
    cnt = __builtin_amdgcn_readfirstlane(cnt);
    CBAR();
    if (cnt == 3) {
      const int hb = lane >> 5, l32 = lane & 31;
      f4 sq = *(LAS f4*)(L + L_QPART + l32 * 16);
#pragma unroll
      for (int uu = 1; uu < 4; uu++) sq += *(LAS f4*)(L + L_QPART + uu * 512 + l32 * 16);
      const int qo = QADDR(((hb * 4 + kq) * 4096 + cb * 64 + l32 * 2) * 8);
      __builtin_amdgcn_raw_buffer_store_b128(v4u{__float_as_uint(hb ? sq[1] : sq[0]), tag, __float_as_uint(hb ? sq[3] : sq[2]), tag}, rs, qo, 0, AUX_SC1);
      STAMP(1);
    }
    {
      const int qoff = QADDR(((slot >> 7) * 4 * 4096 + ((slot >> 4) & 7) * 512 + wv * 128 + lane * 2) * 8);
      __builtin_amdgcn_s_sleep(QSLEEP);
      STAMP(28);
      v4u qg[4];
      _Pragma("clang loop unroll(disable)") for (int spins = 0; spins < SPIN_CAP; ++spins) {
        CBAR();
#pragma unroll
        for (int q = 0; q < 4; q++) { if (q < QFAKE) qg[q] = __builtin_amdgcn_raw_buffer_load_b128(rs, qoff + q * 4096 * 8, 0, AUX_SC1); else qg[q] = qg[0]; }
        bool ok = true;
#pragma unroll
        for (int q = 0; q < 4; q++) ok = ok & (qg[q].y == tag) & (qg[q].w == tag);
        if (__all(ok)) break;
      }
      const float qa = (__uint_as_float(qg[0].x) + __uint_as_float(qg[1].x)) + (__uint_as_float(qg[2].x) + __uint_as_float(qg[3].x));
      const float qb = (__uint_as_float(qg[0].z) + __uint_as_float(qg[1].z)) + (__uint_as_float(qg[2].z) + __uint_as_float(qg[3].z));
      *(LAS u32*)(L + L_QS + wv * 256 + lane * 4) = cvtpk(qa, qb);
      lds_add(SY + C_Q, lane);
      STAMP(29);
    }
    lds_wait_ge(SY + F_K + wv, 3);
  }
  OPAQUE(wv);
  if (wv == 8) {
    if (lane == 0) *(LAS u64*)(SY + W_E) = E0;
    lds_set(SY + F_E, lane);
    VM0();
    if (PRET & 2) { CLEARS(); VM0(); }
    if (lane == 0) __hip_atomic_store((GAS u64*)(a.ws + WS_FLAGS) + w, E0 + 1, __ATOMIC_RELAXED, __HIP_MEMORY_SCOPE_AGENT);
    STAMP(22);
  }
  OPAQUE(wv);
  if (wv >= 8) {
    const int u = wv - 8;
    __builtin_amdgcn_s_setprio(1);
    lds_wait_ge(SY + F_K + (u & 3), 1 + (u >> 2));
    v4u V[16];
    {
      const __amdgpu_buffer_rsrc_t rv = mkrsrc(a.v + (size_t)slot * 512 * DH, 131072);
      const int vo = u * 1024 + lane * 16;
#pragma unroll
      for (int t = 0; t < 16; t++) {
        if (t >= VD && !isFFN) asm volatile("s_waitcnt vmcnt(%0)" :: "n"(VD - 1) : "memory");
        if (t >= VDF && isFFN) asm volatile("s_waitcnt vmcnt(%0)" :: "n"(VDF - 1) : "memory");
        V[t] = __builtin_amdgcn_raw_buffer_load_b128(rv, vo, ((t + rot) & 15) * 8192, VAUX);
        __builtin_amdgcn_sched_barrier(0);
      }
    }
    __builtin_amdgcn_s_setprio(3);
    lds_add(SY + C_VISS, lane);
    STAMP(2);
    lds_wait_ge(SY + F_P + u, 1);
    f4 acc[8];
#pragma unroll
    for (int G = 0; G < 2; G++) {
      const v4u pf = *(LAS v4u*)(L + L_PFR + ((u * 2 + G) * 16 + g * 4 + (c & 3)) * 16);
#pragma unroll
      for (int d2 = 0; d2 < 4; d2++) {
        v4u lo, hi;
#pragma unroll
        for (int t2 = 0; t2 < 4; t2++) {
          const u32 va = V[8 * G + 2 * t2][d2], vb = V[8 * G + 2 * t2 + 1][d2];
          lo[t2] = __builtin_amdgcn_perm(vb, va, 0x05040100u);
          hi[t2] = __builtin_amdgcn_perm(vb, va, 0x07060302u);
        }
        if (G == 0) {
          acc[2 * d2] = mfma16(pf, lo, f4{0.f, 0.f, 0.f, 0.f});
          acc[2 * d2 + 1] = mfma16(pf, hi, f4{0.f, 0.f, 0.f, 0.f});
        } else {
          acc[2 * d2] = mfma16(pf, lo, acc[2 * d2]);
          acc[2 * d2 + 1] = mfma16(pf, hi, acc[2 * d2 + 1]);
        }
      }
      __builtin_amdgcn_sched_barrier(0);
    }
    STAMP(3);
    if (lane < 16) {
#pragma unroll
      for (int dd = 0; dd < 8; dd++) *(LAS f4*)(L + L_MRG + u * 2048 + dd * 256 + c * 16) = acc[dd];
    }
    lds_add(SY + C_O, lane);
    if (u < 2) {
      lds_wait_ge(SY + F_FAC, 1);
      f4 fac[8];
#pragma unroll
      for (int uu = 0; uu < 8; uu++) fac[uu] = *(LAS f4*)(L + L_FAC + uu * 16);
      lds_spin_ge(SY + C_O, 8);
      STAMP(20);
      f4 sacc = *(LAS f4*)(L + L_MRG + (u * 64 + lane) * 16) * fac[0];
#pragma unroll
      for (int uu = 1; uu < 8; uu++) sacc += *(LAS f4*)(L + L_MRG + uu * 2048 + (u * 64 + lane) * 16) * fac[uu];
      __builtin_amdgcn_raw_buffer_store_b128(canon4(sacc), rs, WS_MB1 + slot * 2048 + (u * 64 + lane) * 16, 0, AUX_SC1);
      lds_add(SY + C_PUB, lane);
      STAMP(4);
    }
    if (isWO) {
      const int bq = u >> 2;
      const int off = WS_MB1 + ((bq * 8 + x8) * 16 + g) * 2048 + (wi & 1) * 1024 + (u & 3) * 256 + c * 16;
      lds_wait_ge(SY + F_WT, 1);
      f4 wt[4];
#pragma unroll
      for (int i = 0; i < 4; i++) wt[i] = *(LAS f4*)(L + L_WT + (bq * 16 + g + 4 * i) * 16);
      v4u a0, a1, a2, a3;
      STAMP(31);
      poll4(rs, off, off + 4 * 2048, off + 8 * 2048, off + 12 * 2048, a0, a1, a2, a3);
      const float o = rscatter((F4(a0) * wt[0] + F4(a1) * wt[1]) + (F4(a2) * wt[2] + F4(a3) * wt[3]));
      *(LAS u16*)(L + L_OB + bq * 512 + ((u & 3) * 64 + c * 4 + g) * 2) = (u16)cvtpk(o, o);
      lds_add(SY + C_H, lane);
      STAMP(5);
      if (u == 0 && j < 8) {
        const int fi = x8 * 8 + j, bb = fi >> 5, tile = fi & 31;
        const int q4 = lane & 3, p16 = lane >> 2;
        const u32 xv = *(const GAS u16*)(a.x + bb * DM + tile * 16 + q4 * 4 + g);
        const int o2 = WS_MB2 + ((bb * 16 + p16) * 512 + tile * 16 + q4 * 4) * 4;
        v4u at;
        _Pragma("clang loop unroll(disable)") for (int spins = 0; spins < SPIN_CAP; ++spins) {
          CBAR();
          at = __builtin_amdgcn_raw_buffer_load_b128(rs, o2, 0, AUX_SC1);
          if (__all(valid4(at))) break;
        }
        float t = rscatter(F4(at));
        t += dpp<ROR4>(t);
        t += dpp<XOR8>(t);
        const float hv = rbf(bflo(xv) + rbf(t));
        const int o3 = WS_MB3 + (bb * 32 + tile) * 56 * 64 + lane * 16;
        const bool has3 = lane < 32;
        poll4(rs, o3, o3 + 1024, o3 + 2048, has3 ? o3 + 3072 : o3, a0, a1, a2, a3);
        f4 ds = (F4(a0) + F4(a1)) + F4(a2);
        if (has3) ds += F4(a3);
        t = rscatter(ds);
        t += dpp<ROR4>(t);
        t += dpp<XOR8>(t);
        const float ov = hv + rbf(t);
        if (c < 4) *(GAS u16*)(a.out + bb * DM + tile * 16 + q4 * 4 + g) = (u16)cvtpk(ov, ov);
        STAMP(9);
      }
    } else if (isFFN) {
      const u32 xv = *(const GAS u16*)(a.x + fb * DM + u * 64 + c * 4 + g);
      const int off = WS_MB2 + ((fb * 16 + g) * 512 + u * 64 + c * 4) * 4;
      lds_wait_ge(SY + F_FL, 1);
      lds_wait_ge(SY + C_W, 4);
      v4u bd[4];
      {
        const int r0_ = 8 * g + (c >> 2);
        const int t0_ = (r0_ & 3) | ((g & 1) << 2);
#pragma unroll
        for (int tt = 0; tt < 4; tt++) {
          const int ct = u * 4 + tt;
          v2u b0 = trread(L + L_DN + r0_ * 1024 + ((ct ^ t0_) * 32) + (c & 3) * 8);
          v2u b1 = trread(L + L_DN + (r0_ + 4) * 1024 + ((ct ^ t0_) * 32) + (c & 3) * 8);
          bd[tt] = v4u{b0.x, b0.y, b1.x, b1.y};
        }
      }
      v4u a0, a1, a2, a3;
      STAMP(30);
      poll4(rs, off, off + 4 * 2048, off + 8 * 2048, off + 12 * 2048, a0, a1, a2, a3);
      const float at = rscatter((F4(a0) + F4(a1)) + (F4(a2) + F4(a3)));
      const float hs = bflo(xv) + rbf(at);
      *(LAS u16*)(L + L_OB + (u * 64 + c * 4 + g) * 2) = (u16)cvtpk(hs, hs);
      lds_add(SY + C_H, lane);
      STAMP(6);
      lds_spin_ge(SY + C_A, 4);
      const v4u af = *(LAS v4u*)(L + L_ACT + g * 16);
      const int o3 = c == 0 ? WS_MB3 + ((fb * 32 + u * 4) * 56 + ff) * 64 + g * 16 : OOB;
#pragma unroll
      for (int tt = 0; tt < 4; tt++) {
        const f4 d = mfma16(bd[tt], af, f4{0.f, 0.f, 0.f, 0.f});
        __builtin_amdgcn_raw_buffer_store_b128(canon4(d), rs, o3, tt * 56 * 64, AUX_SC1);
      }
      STAMP(8);
    }
  }
  OPAQUE(wv);
  if (wv < 8) {
    const int u = wv;
    lds_wait_ge(SY + C_Q, 4);
    STAMP(11);
    {
      v4u qf[4];
#pragma unroll
      for (int kk = 0; kk < 4; kk++) qf[kk] = *(LAS v4u*)(L + L_QS + (c & 3) * 256 + (32 * kk + 8 * g) * 2);
      float sc[16];
#pragma unroll
      for (int T = 0; T < 4; T++) {
        f4 d = {0.f, 0.f, 0.f, 0.f};
        const int row = 16 * T + 4 * (c & 3) + (c >> 2);
#pragma unroll
        for (int kk = 0; kk < 4; kk++) {
          const v4u kf = *(LAS v4u*)(L + L_KIMG + (64 * u + row) * 256 + (((4 * kk + g) ^ c) * 16));
          d = mfma16(kf, qf[kk], d);
        }
#pragma unroll
        for (int i = 0; i < 4; i++) sc[4 * T + i] = rbf(rbf(d[i]) * 0.08838834764831845f);
      }
      float m = sc[0];
#pragma unroll
      for (int i = 1; i < 16; i++) m = fmaxf(m, sc[i]);
      m = x16_max(m);
      m = x32_max(m);
      float l = 0.f;
#pragma unroll
      for (int i = 0; i < 16; i++) { sc[i] = __builtin_amdgcn_exp2f((sc[i] - m) * 1.4426950408889634f); l += sc[i]; }
      l = x16_sum(l);
      l = x32_sum(l);
      if (c < 4) {
#pragma unroll
        for (int G = 0; G < 2; G++) {
          v4u pf = {cvtpk(sc[8 * G], sc[8 * G + 1]), cvtpk(sc[8 * G + 2], sc[8 * G + 3]), cvtpk(sc[8 * G + 4], sc[8 * G + 5]), cvtpk(sc[8 * G + 6], sc[8 * G + 7])};
          *(LAS v4u*)(L + L_PFR + ((u * 2 + G) * 16 + g * 4 + c) * 16) = pf;
        }
        if (g == 0) *(LAS f2*)(L + L_ML + (u * 4 + c) * 8) = f2{m, l};
      }
      lds_set(SY + F_P + u, lane);
      lds_add(SY + C_ML, lane);
    }
    STAMP(13);
    if (u == 1) {
      lds_wait_ge(SY + C_ML, 8);
      const int h = lane & 3, uu = (lane >> 2) & 7;
      const f2 ml = *(LAS f2*)(L + L_ML + (uu * 4 + h) * 8);
      float mw = ml[0];
      mw = fmaxf(mw, x4_get(mw)); mw = fmaxf(mw, dpp<XOR8>(mw)); mw = x16_max(mw);
      const float fac = __builtin_amdgcn_exp2f((ml[0] - mw) * 1.4426950408889634f);
      float lw = ml[1] * fac;
      lw += x4_get(lw); lw += dpp<XOR8>(lw); lw = x16_sum(lw);
      if (lane < 4) __builtin_amdgcn_raw_buffer_store_b64(v2u{canon(mw), canon(lw)}, rs, WS_ST + slot * 32 + lane * 8, 0, AUX_SC1);
      if (lane < 32) *(LAS float*)(L + L_FAC + lane * 4) = fac;
      lds_set(SY + F_FAC, lane);
      STAMP(14);
    }
    if (u == 0) {
      const u64 E = *(LAS u64*)(SY + W_E);
      const u32 elo = (u32)(E + 1), ehi = (u32)((E + 1) >> 32);
      _Pragma("clang loop unroll(disable)") for (int spins = 0; spins < SPIN_CAP; ++spins) {
        CBAR();
        const v4u f0 = __builtin_amdgcn_raw_buffer_load_b128(rs, WS_FLAGS + lane * 16, 0, AUX_SC1);
        const v4u f1 = __builtin_amdgcn_raw_buffer_load_b128(rs, WS_FLAGS + 1024 + lane * 16, 0, AUX_SC1);
        const bool ok = (f0.x == elo) & (f0.y == ehi) & (f0.z == elo) & (f0.w == ehi) & (f1.x == elo) & (f1.y == ehi) & (f1.z == elo) & (f1.w == ehi);
        if (__all(ok)) break;
      }
      if (w == 0 && lane == 0) __hip_atomic_store((GAS u64*)(a.ws + WS_EPOCH), E + 1, __ATOMIC_RELAXED, __HIP_MEMORY_SCOPE_AGENT);
      lds_set(SY + F_FL, lane);
      STAMP(17);
    }
    if (isWO) {
      if (u == 0) {
        v2u st0, st1;
        if (STWAIT) lds_wait_ge(SY + C_VISS, 8);
        const int soff = WS_ST + (x8 * 16) * 32 + lane * 8;
        _Pragma("clang loop unroll(disable)") for (int spins = 0; spins < SPIN_CAP; ++spins) {
          CBAR();
          st0 = __builtin_amdgcn_raw_buffer_load_b64(rs, soff, 0, AUX_SC1);
          st1 = __builtin_amdgcn_raw_buffer_load_b64(rs, soff + 128 * 32, 0, AUX_SC1);
          if (__all((st0.x != SENT) & (st0.y != SENT) & (st1.x != SENT) & (st1.y != SENT))) break;
          __builtin_amdgcn_s_sleep(STSLEEP);
        }
#pragma unroll
        for (int bb = 0; bb < 2; bb++) {
          const float ms = __uint_as_float(bb ? st1.x : st0.x), ls = __uint_as_float(bb ? st1.y : st0.y);
          float M = ms;
          M = fmaxf(M, x4_get(M)); M = fmaxf(M, dpp<XOR8>(M)); M = x16_max(M); M = x32_max(M);
          const float ex = __builtin_amdgcn_exp2f((ms - M) * 1.4426950408889634f);
          float Ls = ls * ex;
          Ls += x4_get(Ls); Ls += dpp<XOR8>(Ls); Ls = x16_sum(Ls); Ls = x32_sum(Ls);
          *(LAS float*)(L + L_WT + bb * 256 + lane * 4) = ex / Ls;
        }
        lds_set(SY + F_WT, lane);
        STAMP(18);
      }
      if (u >= 4) {
        lds_wait_ge(SY + C_ML, 8);
        if (WLATE & 1) lds_wait_ge(SY + C_PUB, 2); else lds_wait_ge(SY + C_VISS, 8);
        __builtin_amdgcn_s_setprio(0);
        const __amdgpu_buffer_rsrc_t rw = mkrsrc(a.wo, 4096 * DM * 2);
        const u32 vo = (u32)(((r8 & 3) * 128 + 8 * (r8 >> 2)) * 1024 + wjj * 128);
        dma_run2<1>(rw, vo + swA, vo + swB, (u32)((x8 * 512 + 4 * (wi & 1) + (u - 4)) * 1024), 16 * 1024, (u32)(size_t)(L + L_WO) + (u - 4) * 8192, 4);
        VM0();
        lds_add(SY + C_W, lane);
        STAMP(15);
      } else {
        lds_wait_ge(SY + C_W, 4);
        v4u bf[8];
        const int r0_ = 8 * g + (c >> 2);
        const int sg0 = ((r0_ >> 1) & 1) | ((g & 1) << 1);
        LAS char* bp = L + L_WO + r0_ * 128 + ((u ^ sg0) * 32) + (c & 3) * 8;
#pragma unroll
        for (int ks = 0; ks < 8; ks++) {
          v2u b0 = trread(bp + ks * 4096), b1 = trread(bp + ks * 4096 + 512);
          bf[ks] = v4u{b0.x, b0.y, b1.x, b1.y};
        }
        STAMP(19);
        lds_spin_ge(SY + C_H, 8);
        STAMP(21);
        f4 d = {0.f, 0.f, 0.f, 0.f};
#pragma unroll
        for (int ks = 0; ks < 8; ks++) {
          const v4u of = *(LAS v4u*)(L + L_OB + (c & 1) * 512 + (32 * ks + 8 * g) * 2);
          d = mfma16(bf[ks], of, d);
        }
        __builtin_amdgcn_raw_buffer_store_b128(canon4(d), rs, c < 2 ? WS_MB2 + ((c * 16 + wi) * 512 + wjj * 64 + u * 16 + g * 4) * 4 : OOB, 0, AUX_SC1);
        STAMP(16);
      }
    } else if (isFFN) {
      if (u >= 4) {
        lds_wait_ge(SY + C_ML, 8);
        if (WSTART) lds_wait_ge(SY + C_VISS, 8);
        __builtin_amdgcn_s_setprio(0);
        const int lam = u - 4;
        const __amdgpu_buffer_rsrc_t rg = mkrsrc(a.wg, DM * FF * 2), ru = mkrsrc(a.wu, DM * FF * 2), rd = mkrsrc(a.wd, FF * DM * 2);
        const int r16 = lane >> 2, q4 = lane & 3;
        const u32 vg = (u32)(r16 * FF * 2 + ff * 64 + ((q4 ^ (r16 >> 3)) * 16)), vu = (u32)(r16 * FF * 2 + ff * 64 + ((q4 ^ ((r16 >> 3) | 2)) * 16));
        u32 mode = 0;
#define PACE() do { if (mode == 0) { asm volatile("s_waitcnt vmcnt(%0)" :: "n"(WD1 - 1) : "memory"); \
                      mode = __builtin_amdgcn_readfirstlane(__hip_atomic_load(SY + C_PUB, __ATOMIC_RELAXED, __HIP_MEMORY_SCOPE_WORKGROUP)) >= 2; } } while (0)
        _Pragma("clang loop unroll(disable)") for (int t = 0; t < 8; t++) {
          const u32 P = lam * 8 + t;
          PACE();
          bl_lds(rg, vg, P * (16 * FF * 2), (u32)(size_t)(L + L_GU) + P * 1024);
          PACE();
          bl_lds(ru, vu, P * (16 * FF * 2), (u32)(size_t)(L + L_GU) + 32768 + P * 1024);
        }
        _Pragma("clang loop unroll(disable)") for (int t = 0; t < 8; t++) {
          const u32 k = lam * 8 + t;
          const u32 tau = (k & 3) | (((k >> 3) & 1) << 2);
          PACE();
          bl_lds(rd, (u32)((((lane >> 1) ^ tau) * 2 + (lane & 1)) * 16), (ff * 32 + k) * 1024, (u32)(size_t)(L + L_DN) + k * 1024);
        }
        VM0();
        lds_add(SY + C_W, lane);
        STAMP(15);
      } else {
        lds_wait_ge(SY + C_W, 4);
        v4u bfr[16];
        const int mu = (c >> 1) & 1;
        LAS char* bp = L + L_GU + mu * 32768 + (8 * g + (c >> 2)) * 64 + ((u ^ ((g & 1) | (mu << 1))) * 16) + (c & 1) * 8;
        f4 cs = {0.f, 0.f, 0.f, 0.f};
        const v4u ones = {0x3F803F80u, 0x3F803F80u, 0x3F803F80u, 0x3F803F80u};
#pragma unroll
        for (int ks = 0; ks < 16; ks++) {
          v2u b0 = trread(bp + ks * 2048), b1 = trread(bp + ks * 2048 + 256);
          bfr[ks] = v4u{b0.x, b0.y, b1.x, b1.y};
          cs = mfma16(ones, bfr[ks], cs);
        }
        const float colsum = cs[0];
        STAMP(23);
        lds_spin_ge(SY + C_H, 8);
        STAMP(24);
        const v4u hv = *(LAS v4u*)(L + L_OB + lane * 16);
        f4 d = {0.f, 0.f, 0.f, 0.f};
#pragma unroll
        for (int ks = 0; ks < 16; ks++) {
          const v4u af = *(LAS v4u*)(L + L_OB + (32 * ks + 8 * g) * 2);
          d = mfma16(af, bfr[ks], d);
        }
        float sm = 0.f;
#pragma unroll
        for (int e = 0; e < 4; e++) sm += bflo(hv[e]) + bfhi(hv[e]);
        const float mean = wave_sum(sm) * (1.0f / DM);
        float q2 = 0.f;
#pragma unroll
        for (int e = 0; e < 4; e++) {
          float t = bflo(hv[e]) - mean; q2 = fmaf(t, t, q2);
          t = bfhi(hv[e]) - mean; q2 = fmaf(t, t, q2);
        }
        q2 = wave_sum(q2);
        const float m = rbf(mean), vv = rbf(q2 * (1.0f / DM));
        const float den = rbf(sqrtf(rbf(vv + 1e-5f)));
        const float pre = rbf((d[0] - m * colsum) / den);
        const float other = dpp<XOR8>(pre);
        const float sg = rbf(pre / (1.0f + __expf(-pre)));
        const float act = sg * other;
        if (lane < 8) *(LAS u16*)(L + L_ACT + (u * 8 + lane) * 2) = (u16)cvtpk(act, act);
        lds_add(SY + C_A, lane);
        STAMP(7);
      }
    }
  }
  FLUSH_TS();
}

extern "C" void kernel_launch(void* const* d_in, const int* in_sizes, int n_in, void* d_out, int out_size,
                              void* d_ws, size_t ws_size, hipStream_t stream) {
  Args a;
  a.ws = (char*)d_ws;
  a.x = (const u16*)d_in[0];
  a.k = (const u16*)d_in[1];
  a.v = (const u16*)d_in[2];
  a.wq = (const u16*)d_in[3];
  a.wo = (const u16*)d_in[4];
  a.wg = (const u16*)d_in[6];
  a.wu = (const u16*)d_in[7];
  a.wd = (const u16*)d_in[8];
  a.out = (u16*)d_out;
  hipLaunchKernelGGL(fused_block, dim3(NWG), dim3(NT), 0, stream, a);
}
```

```cpp
#include <hip/hip_runtime.h>
#include <stdint.h>

typedef unsigned short u16;
typedef unsigned int u32;
typedef unsigned long long u64;
typedef u32 v4u __attribute__((ext_vector_type(4)));
typedef u32 v2u __attribute__((ext_vector_type(2)));
typedef float f4 __attribute__((ext_vector_type(4)));
typedef float f2 __attribute__((ext_vector_type(2)));
typedef short s4v __attribute__((ext_vector_type(4)));
typedef __bf16 bf8 __attribute__((ext_vector_type(8)));
typedef __bf16 bf2 __attribute__((ext_vector_type(2)));
#define GAS __attribute__((address_space(1)))
#define LAS __attribute__((address_space(3)))

#define DEBUG_TS 0
#define KD 8
#define KPRE 0
#define VDU 4
#define KREL 2
#define QSLEEP 56
#define STSLEEP 8
#define WD1 3
#define FGATE 2
#define PFWO 0
#define FLAT 16
#define FLSLEEP 2
#define PPN 4
#define PPSLEEP 2
#define PPDELAY 20
#define RSLEEP 8
#define ONE1 1
#define ONE2 1
#define PSL 1
#define M2POS 1
#define M3POS 2
#define MKD 0
#define RDELAY 20
#define M1W 4
#define RST 2
#define STF 1
#define PT 0
#define NOQ 0
#define NOST 0
#define SW 0
#define SN 0
#define Q4 1
#define CPF 0
#define EX2 2
#define E2D 6
#define LBAR 0
#define MSC 1
#define QLOW 1
#define VPT 0
#define PSC " sc1"
#define T1 20
#define T2 8
#define XD 4
#define TS 20
#define TF 28
#define PFM 0
#define LPRIO 3
#define GSLEEP 1
#define GPRIO 2
#define EPF 0
#define PFD 0
#define TWO 1
#define S0 12
#define M0 12
#define C3PF 3
#define P4 1
#define HP 1
#define M3S 2
#define M1S 0
#define M2S 0
#define PFOLD 3
#define MKE 0
#define VP2 0
#define SPT 1
#define SPD 40
#define KA 0
#define CODE_LEN 20224
#undef CODE_KB
#define CODE_KB (CODE_LEN / 1024)

constexpr int DM = 512, DH = 128, FF = 1792;
constexpr int NWG = 256, NT = 1024;
constexpr int WS_EPOCH = 0;
constexpr int WS_ST = 4096;
constexpr int STP = STF ? 128 : 64;
constexpr int WS_MB1 = WS_ST + 256 * 128;
constexpr int WS_MB2 = WS_MB1 + 256 * 2048;
constexpr int WS_MB3 = WS_MB2 + 2 * 16 * 2048;
constexpr int WS_FL = WS_MB3 + 2 * 8 * 2 * 56 * 128;
constexpr int WS_M3 = WS_FL + 256 * 128;
constexpr int WS_CT = WS_M3 + 256 * 128;
constexpr int WS_QG = 2 << 20;
constexpr int WS_END = WS_QG + 9 * 262144;
constexpr int WS_DBG = (WS_END + 4095) / 4096 * 4096;
#define QADDR(off) (QLOW ? (1 << 20) + (off) + (((off) >> 12) & 7) * 131072 : WS_QG + (off) + (((off) >> 12) & 7) * 262144)
constexpr int AUX_SC1 = 16;
constexpr int OOB = 0x7FFFFFF0;
constexpr int SPIN_CAP = 1 << 16;
constexpr int L_KIMG = 0;
constexpr int L_WO = 0;
constexpr int L_GU = 0;
constexpr int L_DN = 65536;
constexpr int L_WQ = 131072;
constexpr int L_MRG = L_WQ;
constexpr int L_PFR = L_WQ + 16384;
constexpr int L_QPART = L_PFR + 4096;
constexpr int L_QS = L_QPART + 2048;
constexpr int L_OB = L_QS + 1024;
constexpr int L_WP = L_OB + 1024;
constexpr int L_CS = L_WP + 2048;
constexpr int L_ACT = L_CS + 2048;
constexpr int L_ML = L_ACT + 1024;
constexpr int L_LS = L_ML + 128;
constexpr int L_STAT = L_LS + 128;
constexpr int L_WT = L_STAT + 64;
constexpr int L_ONES = L_WT + 512;
constexpr int L_SYNC = L_ONES + 128;
constexpr int L_DBG = L_SYNC + 256;
constexpr int L_DUMMY = L_DBG + 256;
constexpr int LDS_BYTES = L_DUMMY + 1024;
static_assert(LDS_BYTES <= 163840, "lds");
enum { C_QP = 0, C_Q = 1, C_ML = 2, C_M = 3, C_VISS = 4, C_W = 5, F_E = 6, C_VL = 7, F_P = 8  , W_E = 16  ,
       C_PUB = 18, C_O = 19, F_WT = 20, C_H = 21, C_CS = 22, C_KL = 23, F_K = 24  , C_WO = 28  , C_PUB2 = 30, F_GO = 31, F_MK = 32, F_NM = 33, W_B = 34  , C_G = 41  , C_A = 43, C_F = 44, C_X = 45, W_Z = 48   };

struct Args {
  char* ws;
  const u16 *x, *wq, *k, *v, *wo, *wg, *wu, *wd;
  u16* out;
};

__device__ __forceinline__ float bflo(u32 v) { return __uint_as_float(v << 16); }
__device__ __forceinline__ float bfhi(u32 v) { return __uint_as_float(v & 0xFFFF0000u); }
__device__ __forceinline__ u32 cvtpk(float lo, float hi) {
  return __builtin_bit_cast(u32, __builtin_convertvector(f2{lo, hi}, bf2));
}
__device__ __forceinline__ float rbf(float f) { return bflo(cvtpk(f, f)); }
template <int CTRL> __device__ __forceinline__ float dpp(float x) {
  return __builtin_bit_cast(float, __builtin_amdgcn_mov_dpp(__builtin_bit_cast(int, x), CTRL, 0xf, 0xf, true));
}
constexpr int XOR1 = 0xB1, XOR2 = 0x4E, XOR7 = 0x141, XOR8 = 0x128, ROR4 = 0x124;
__device__ __forceinline__ float x16_sum(float x) {
  auto s = __builtin_amdgcn_permlane16_swap(__float_as_uint(x), __float_as_uint(x), false, false);
  return __uint_as_float(s[0]) + __uint_as_float(s[1]);
}
__device__ __forceinline__ float x32_sum(float x) {
  auto s = __builtin_amdgcn_permlane32_swap(__float_as_uint(x), __float_as_uint(x), false, false);
  return __uint_as_float(s[0]) + __uint_as_float(s[1]);
}
__device__ __forceinline__ float x16_max(float x) {
  auto s = __builtin_amdgcn_permlane16_swap(__float_as_uint(x), __float_as_uint(x), false, false);
  return fmaxf(__uint_as_float(s[0]), __uint_as_float(s[1]));
}
__device__ __forceinline__ float x32_max(float x) {
  auto s = __builtin_amdgcn_permlane32_swap(__float_as_uint(x), __float_as_uint(x), false, false);
  return fmaxf(__uint_as_float(s[0]), __uint_as_float(s[1]));
}
__device__ __forceinline__ float x4_get(float x) { return dpp<XOR7>(dpp<0x1B>(x)); }
__device__ __forceinline__ float wave_sum(float s) {
  s += dpp<XOR1>(s); s += dpp<XOR2>(s); s += dpp<XOR7>(s); s += dpp<XOR8>(s);
  s = x16_sum(s);
  return x32_sum(s);
}
__device__ __forceinline__ float rscatter(f4 v) {
  auto p = __builtin_amdgcn_permlane32_swap(__float_as_uint(v[0]), __float_as_uint(v[2]), false, false);
  auto q = __builtin_amdgcn_permlane32_swap(__float_as_uint(v[1]), __float_as_uint(v[3]), false, false);
  const float ps = __uint_as_float(p[0]) + __uint_as_float(p[1]);
  const float qs = __uint_as_float(q[0]) + __uint_as_float(q[1]);
  auto r = __builtin_amdgcn_permlane16_swap(__float_as_uint(ps), __float_as_uint(qs), false, false);
  return __uint_as_float(r[0]) + __uint_as_float(r[1]);
}
__device__ __forceinline__ f4 mfma16(v4u a, v4u b, f4 c) {
  return __builtin_amdgcn_mfma_f32_16x16x32_bf16(__builtin_bit_cast(bf8, a), __builtin_bit_cast(bf8, b), c, 0, 0, 0);
}
__device__ __forceinline__ v2u trread(LAS char* p) {
  s4v r = __builtin_amdgcn_ds_read_tr16_b64_v4i16((LAS s4v*)p);
  return __builtin_bit_cast(v2u, r);
}
__device__ __forceinline__ u32 enc(float f, u32 tg) {
  return __builtin_amdgcn_bitop3_b32(__float_as_uint(f) + 0x200u, ~0x3FFu, tg, 0xEA);
}
__device__ __forceinline__ v4u enc4(f4 v, u32 tg) { return v4u{enc(v[0], tg), enc(v[1], tg), enc(v[2], tg), enc(v[3], tg)}; }
__device__ __forceinline__ bool ok4(v4u v) { return ((v.x | v.y | v.z | v.w) & 0x3FFu) == 0; }
#define LGKM0() asm volatile("s_waitcnt lgkmcnt(0)" ::: "memory")
#define VM0() asm volatile("s_waitcnt vmcnt(0)" ::: "memory")
#define CBAR() asm volatile("" ::: "memory")
#define OPAQUE(x) asm volatile("" : "+s"(x))
#define PIN(x) do { if (HP) asm volatile("" : "+v"(x)); } while (0)
#define LP(T, adr) ((LAS T*)(size_t)(adr))
__device__ __forceinline__ void wg_barrier() {
  __builtin_amdgcn_sched_barrier(0);
  LGKM0();
  __builtin_amdgcn_s_barrier();
  CBAR();
  __builtin_amdgcn_sched_barrier(0);
}
__device__ __forceinline__ void lds_wait_ge(LAS u32* p, u32 want) {
  _Pragma("clang loop unroll(disable)") for (int spins = 0; spins < SPIN_CAP; ++spins) {
    u32 v = __hip_atomic_load(p, __ATOMIC_RELAXED, __HIP_MEMORY_SCOPE_WORKGROUP);
    if (__builtin_amdgcn_readfirstlane(v) >= want) break;
    __builtin_amdgcn_s_sleep(1);
  }
  CBAR();
}
__device__ __forceinline__ void lds_spin_ge(LAS u32* p, u32 want) {
  _Pragma("clang loop unroll(disable)") for (int spins = 0; spins < SPIN_CAP * 16; ++spins) {
    u32 v = __hip_atomic_load(p, __ATOMIC_RELAXED, __HIP_MEMORY_SCOPE_WORKGROUP);
    if (__builtin_amdgcn_readfirstlane(v) >= want) break;
  }
  CBAR();
}
__device__ __forceinline__ void lds_add(LAS u32* p, int) {
  u64 keep;
  asm volatile("s_mov_b64 %0, exec\n\ts_mov_b64 exec, 1\n\tds_add_u32 %1, %2\n\ts_mov_b64 exec, %0" : "=&s"(keep) : "v"((u32)(size_t)p), "v"(1u) : "memory");
}
__device__ __forceinline__ u32 lds_add_ret(LAS u32* p, int) {
  u64 keep;
  u32 r;
  asm volatile("s_mov_b64 %0, exec\n\ts_mov_b64 exec, 1\n\tds_add_rtn_u32 %1, %2, %3\n\ts_mov_b64 exec, %0\n\ts_waitcnt lgkmcnt(0)"
               : "=&s"(keep), "=&v"(r) : "v"((u32)(size_t)p), "v"(1u) : "memory");
  return __builtin_amdgcn_readfirstlane(r);
}
__device__ __forceinline__ u32 lds_add_read4(u32 cnt, u32 adr, f4& r0, f4& r1, f4& r2, f4& r3) {
  u64 keep;
  u32 r;
  asm volatile("s_mov_b64 %0, exec\n\ts_mov_b64 exec, 1\n\tds_add_rtn_u32 %1, %6, %7\n\ts_mov_b64 exec, 0xffff\n\t"
               "ds_read_b128 %2, %8\n\tds_read_b128 %3, %8 offset:256\n\tds_read_b128 %4, %8 offset:512\n\tds_read_b128 %5, %8 offset:768\n\t"
               "s_mov_b64 exec, %0\n\ts_waitcnt lgkmcnt(0)"
               : "=&s"(keep), "=&v"(r), "=&v"(r0), "=&v"(r1), "=&v"(r2), "=&v"(r3) : "v"(cnt), "v"(1u), "v"(adr) : "memory");
  return __builtin_amdgcn_readfirstlane(r);
}
__device__ __forceinline__ void lds_set(LAS u32* p, int, u32 v) {
  u64 keep;
  asm volatile("s_mov_b64 %0, exec\n\ts_mov_b64 exec, 1\n\tds_write_b32 %1, %2\n\ts_mov_b64 exec, %0" : "=&s"(keep) : "v"((u32)(size_t)p), "v"(v) : "memory");
}
__device__ __forceinline__ void glds16(const void* gsrc, u32 lds_dst) {
  u32 keep;
  asm volatile("s_mov_b32 %0, m0\n\ts_mov_b32 m0, %2\n\ts_nop 0\n\tglobal_load_lds_dwordx4 %1, off\n\ts_mov_b32 m0, %0"
               : "=&s"(keep) : "v"(gsrc), "s"(lds_dst) : "memory");
}
template <int NTP> __device__ __forceinline__ void dma_run2(__amdgpu_buffer_rsrc_t r, u32 va, u32 vb, u32 so, u32 gstep, u32 lds0, u32 lstep, u32 npairs) {
  u32 keep;
  if (NTP)
    asm volatile(
        "s_nop 4\n\ts_mov_b32 %[keep], m0\n\ts_mov_b32 m0, %[l0]\n"
        "1:\n\ts_sub_u32 %[cnt], %[cnt], 1\n\t"
        "buffer_load_dwordx4 %[va], %[r], %[so] offen nt lds\n\t"
        "s_add_u32 m0, m0, %[ls]\n\ts_add_u32 %[so], %[so], %[gs]\n\t"
        "buffer_load_dwordx4 %[vb], %[r], %[so] offen nt lds\n\t"
        "s_add_u32 m0, m0, %[ls]\n\ts_add_u32 %[so], %[so], %[gs]\n\t"
        "s_cmp_lg_u32 %[cnt], 0\n\ts_cbranch_scc1 1b\n\ts_mov_b32 m0, %[keep]"
        : [keep] "=&s"(keep), [so] "+s"(so), [cnt] "+s"(npairs)
        : [va] "v"(va), [vb] "v"(vb), [r] "s"(r), [l0] "s"(lds0), [gs] "s"(gstep), [ls] "s"(lstep) : "memory", "scc");
  else
    asm volatile(
        "s_nop 4\n\ts_mov_b32 %[keep], m0\n\ts_mov_b32 m0, %[l0]\n"
        "1:\n\ts_sub_u32 %[cnt], %[cnt], 1\n\t"
        "buffer_load_dwordx4 %[va], %[r], %[so] offen lds\n\t"
        "s_add_u32 m0, m0, %[ls]\n\ts_add_u32 %[so], %[so], %[gs]\n\t"
        "buffer_load_dwordx4 %[vb], %[r], %[so] offen lds\n\t"
        "s_add_u32 m0, m0, %[ls]\n\ts_add_u32 %[so], %[so], %[gs]\n\t"
        "s_cmp_lg_u32 %[cnt], 0\n\ts_cbranch_scc1 1b\n\ts_mov_b32 m0, %[keep]"
        : [keep] "=&s"(keep), [so] "+s"(so), [cnt] "+s"(npairs)
        : [va] "v"(va), [vb] "v"(vb), [r] "s"(r), [l0] "s"(lds0), [gs] "s"(gstep), [ls] "s"(lstep) : "memory", "scc");
}
__device__ __forceinline__ void bl_lds(__amdgpu_buffer_rsrc_t r, u32 vo, u32 so, u32 lds) {
  u32 keep;
  asm volatile("s_mov_b32 %[keep], m0\n\ts_mov_b32 m0, %[l]\n\ts_nop 0\n\tbuffer_load_dwordx4 %[v], %[r], %[so] offen lds\n\ts_mov_b32 m0, %[keep]"
               : [keep] "=&s"(keep) : [v] "v"(vo), [r] "s"(r), [so] "s"(so), [l] "s"(lds) : "memory");
}
__device__ __forceinline__ void kloop(__amdgpu_buffer_rsrc_t rk, u32 vo, u32 so, u32 lds0, u32 cnt) {
  u32 keep;
  asm volatile(
      "s_nop 4\n\ts_mov_b32 %[keep], m0\n\ts_mov_b32 m0, %[l0]\n"
      "1:\n\ts_sub_u32 %[cnt], %[cnt], 1\n\t"
      "s_waitcnt vmcnt(%[kd])\n\t"
      "buffer_load_dwordx4 %[v], %[rk], %[so] offen nt lds\n\t"
      "s_add_u32 m0, m0, 0x1000\n\ts_add_u32 %[so], %[so], 0x1000\n\t"
      "s_cmp_lg_u32 %[cnt], 0\n\ts_cbranch_scc1 1b\n\ts_mov_b32 m0, %[keep]"
      : [keep] "=&s"(keep), [so] "+s"(so), [cnt] "+s"(cnt)
      : [v] "v"(vo), [rk] "s"(rk), [l0] "s"(lds0), [kd] "n"(KD - 1)
      : "memory", "scc");
}
__device__ __forceinline__ __amdgpu_buffer_rsrc_t mkrsrc(const void* p, int bytes) {
  return __builtin_amdgcn_make_buffer_rsrc((void*)p, 0, bytes, 0x00020000);
}

#define STAMP(idx) do {} while (0)
#define FLUSH_TS() do {} while (0)

__device__ __forceinline__ v4u ldx(__amdgpu_buffer_rsrc_t rs, int o, u32 tg) {
  const v4u v = __builtin_amdgcn_raw_buffer_load_b128(rs, o, 0, AUX_SC1);
  return v4u{v.x ^ tg, v.y ^ tg, v.z ^ tg, v.w ^ tg};
}
__device__ __forceinline__ v4u poll1(__amdgpu_buffer_rsrc_t rs, u32 tg, int o) {
  v4u v;
  _Pragma("clang loop unroll(disable)") for (int spins = 0; spins < SPIN_CAP; ++spins) {
    CBAR();
    v = ldx(rs, o, tg);
    if (__all(ok4(v))) break;
  }
  return v;
}
#define F4(x) __builtin_bit_cast(f4, x)
template <int STEP> __device__ __forceinline__ void round4(__amdgpu_buffer_rsrc_t rs, int off, u64 w0, u64 w1, u64 w2, u64 w3, v4u& a0, v4u& a1, v4u& a2, v4u& a3) {
  u64 keep;
  asm volatile(
      "s_mov_b64 %[k], exec\n\t"
      "s_mov_b64 exec, %[w0]\n\ts_cbranch_execz 1f\n\tbuffer_load_dwordx4 %[a0], %[o], %[r], 0 offen sc1\n"
      "1:\n\ts_mov_b64 exec, %[w1]\n\ts_cbranch_execz 2f\n\tbuffer_load_dwordx4 %[a1], %[o], %[r], %[s1] offen sc1\n"
      "2:\n\ts_mov_b64 exec, %[w2]\n\ts_cbranch_execz 3f\n\tbuffer_load_dwordx4 %[a2], %[o], %[r], %[s2] offen sc1\n"
      "3:\n\ts_mov_b64 exec, %[w3]\n\ts_cbranch_execz 4f\n\tbuffer_load_dwordx4 %[a3], %[o], %[r], %[s3] offen sc1\n"
      "4:\n\ts_mov_b64 exec, %[k]\n\ts_waitcnt vmcnt(0)"
      : [k] "=&s"(keep), [a0] "+v"(a0), [a1] "+v"(a1), [a2] "+v"(a2), [a3] "+v"(a3)
      : [o] "v"(off), [r] "s"(rs), [w0] "s"(w0), [w1] "s"(w1), [w2] "s"(w2), [w3] "s"(w3), [s1] "s"(STEP), [s2] "s"(2 * STEP), [s3] "s"(3 * STEP)
      : "memory");
}
__device__ __forceinline__ u64 bad4(v4u v, u32 tg) { return __ballot(((((v.x ^ tg) | (v.y ^ tg)) | ((v.z ^ tg) | (v.w ^ tg))) & 0x3FFu) != 0); }
#define PSTEP(I, W) \
    "global_atomic_or %[" I "], %[a], %[z], off sc0" PSC "\n\t" \
    "s_waitcnt vmcnt(3)\n\t" \
    "v_xor_b32 %[" W "], %[b], %[" W "]\n\t" \
    ".if %[mode] == 1\n\tv_lshrrev_b32 %[t], 16, %[" W "]\n\tv_and_b32 %[t], %[t], %[" W "]\n\tds_write_b32 %[la], %[t]\n\t.endif\n\t" \
    ".if %[mode] == 0\n\tds_write_b32 %[la], %[" W "]\n\t.endif\n\t" \
    "v_cmp_ne_u32 vcc, %[full], %[" W "]\n\t" \
    "s_cbranch_vccz 2f\n\t" \
    "s_sleep %[sl]\n\t"
#define PLOOP \
    "s_mov_b64 %[k], exec\n\ts_mov_b64 exec, %[ex]\n\t" \
    "global_atomic_or %[p0], %[a], %[z], off sc0" PSC "\n\ts_sleep %[sl]\n\t" \
    "global_atomic_or %[p1], %[a], %[z], off sc0" PSC "\n\ts_sleep %[sl]\n\t" \
    "global_atomic_or %[p2], %[a], %[z], off sc0" PSC "\n\ts_sleep %[sl]\n" \
    "1:\n\t" \
    PSTEP("p3", "p0") PSTEP("p0", "p1") PSTEP("p1", "p2") PSTEP("p2", "p3") \
    "s_sub_u32 %[cnt], %[cnt], 1\n\ts_cmp_lg_u32 %[cnt], 0\n\ts_cbranch_scc1 1b\n\t"
template <int MODE, int FULL> __device__ __forceinline__ void watch(const char* p, u32 base, u32 ldsaddr) {
  u64 keep;
  u32 p0, p1, p2, p3, t, cnt = 512;
  asm volatile(
    PLOOP
    "v_mov_b32 %[t], -1\n\tds_write_b32 %[la], %[t]\n"
    "2:\n\ts_mov_b64 exec, %[k]\n\ts_waitcnt vmcnt(0)"
    : [k] "=&s"(keep), [p0] "=&v"(p0), [p1] "=&v"(p1), [p2] "=&v"(p2), [p3] "=&v"(p3), [t] "=&v"(t), [cnt] "+s"(cnt)
    : [a] "v"(p), [b] "v"(base), [z] "v"(0u), [la] "v"(ldsaddr), [sl] "n"(PSL), [ex] "n"(1), [mode] "n"(MODE), [full] "s"(FULL) : "memory", "vcc", "scc");
}
__device__ __forceinline__ void watch3(const char* p, u32 base, __amdgpu_buffer_rsrc_t rs, int o2, int o3, v4u& at, v4u& a0, v4u& a1, v4u& a2, v4u& a3) {
  u64 keep;
  u32 p0, p1, p2, p3, cnt = 512;
  asm volatile(
    PLOOP
    "2:\n\ts_mov_b64 exec, %[k]\n\t"
    "buffer_load_dwordx4 %[a0], %[o3], %[r], 0 offen sc1\n\t"
    "buffer_load_dwordx4 %[a1], %[o3], %[r], 0 offen offset:2048 sc1\n\t"
    "buffer_load_dwordx4 %[a2], %[o3], %[r], %[s2] offen sc1\n\t"
    "buffer_load_dwordx4 %[at], %[o2], %[r], 0 offen sc1\n\t"
    "s_mov_b64 exec, 0xffffffff\n\t"
    "buffer_load_dwordx4 %[a3], %[o3], %[r], %[s2] offen offset:2048 sc1\n\t"
    "s_mov_b64 exec, %[k]\n\ts_waitcnt vmcnt(0)"
    : [k] "=&s"(keep), [p0] "=&v"(p0), [p1] "=&v"(p1), [p2] "=&v"(p2), [p3] "=&v"(p3), [cnt] "+s"(cnt),
      [at] "=&v"(at), [a0] "=&v"(a0), [a1] "=&v"(a1), [a2] "=&v"(a2), [a3] "+v"(a3)
    : [a] "v"(p), [b] "v"(base), [z] "v"(0u), [la] "v"(0u), [t] "v"(0u), [sl] "n"(PSL), [ex] "n"(15), [mode] "n"(2), [full] "s"(0x3FFF), [o2] "v"(o2), [o3] "v"(o3), [r] "s"(rs), [s2] "s"(4096)
    : "memory", "vcc", "scc");
}
template <int FL, int ONE> __device__ __forceinline__ void pgather(__amdgpu_buffer_rsrc_t rs, int off, u32 maddr, u32 faddr, u32 want, u32& last, v4u& a0, v4u& a1, v4u& a2, v4u& a3) {
  u64 keep; u32 vm, vf, m, n, cnt = 1 << 16;
  asm volatile(
    "s_mov_b64 %[k], exec\n\t"
    "s_cmp_eq_u32 %[last], 0xffff\n\ts_cbranch_scc1 3f\n"
    "1:\n\t"
    "ds_read_u16 %[vm], %[la]\n\t"
    ".if %[fl]\n\tds_read_b32 %[vf], %[fa]\n\t.endif\n\t"
    "s_sub_u32 %[cnt], %[cnt], 1\n\ts_cbranch_scc1 3f\n\t"
    "s_waitcnt lgkmcnt(0)\n\t"
    "v_readfirstlane_b32 %[m], %[vm]\n\t"
    ".if %[one]\n\ts_or_b32 %[n], %[m], %[last]\n\ts_cmp_eq_u32 %[n], 0xffff\n\ts_cselect_b32 %[m], %[m], 0\n\t.endif\n\t"
    "s_andn2_b32 %[n], %[m], %[last]\n\t"
    "s_cbranch_scc1 2f\n\t"
    ".if %[fl]\n\tv_readfirstlane_b32 %[m], %[vf]\n\ts_cmp_ge_u32 %[m], %[want]\n\ts_cbranch_scc1 3f\n\t.endif\n\t"
    "s_sleep %[gs]\n\ts_branch 1b\n"
    "2:\n\t"
    "s_bitreplicate_b64_b32 s[90:91], %[n]\n\t"
    "s_or_b32 %[last], %[last], %[n]\n\t"
    "s_bitreplicate_b64_b32 s[90:91], s90\n\t"
    "s_bitreplicate_b64_b32 s[92:93], s90\n\t"
    "s_bitreplicate_b64_b32 s[94:95], s91\n\t"
    "s_bitreplicate_b64_b32 exec, s92\n\ts_cbranch_execz 10f\n\tbuffer_load_dwordx4 %[a0], %[o], %[r], 0 offen sc1\n10:\n\t"
    "s_bitreplicate_b64_b32 exec, s93\n\ts_cbranch_execz 11f\n\tbuffer_load_dwordx4 %[a1], %[o], %[r], %[s1] offen sc1\n11:\n\t"
    "s_bitreplicate_b64_b32 exec, s94\n\ts_cbranch_execz 12f\n\tbuffer_load_dwordx4 %[a2], %[o], %[r], %[s2] offen sc1\n12:\n\t"
    "s_bitreplicate_b64_b32 exec, s95\n\ts_cbranch_execz 13f\n\tbuffer_load_dwordx4 %[a3], %[o], %[r], %[s3] offen sc1\n13:\n\t"
    "s_mov_b64 exec, %[k]\n\t"
    "s_cmp_lg_u32 %[last], 0xffff\n\ts_cbranch_scc1 1b\n"
    "3:\n\t"
    ".if %[fl]\n\ts_waitcnt lgkmcnt(0)\n\t.else\n\ts_setprio 3\n\ts_waitcnt vmcnt(0) lgkmcnt(0)\n\t.endif"
    : [k] "=&s"(keep), [vm] "=&v"(vm), [vf] "=&v"(vf), [m] "=&s"(m), [n] "=&s"(n), [cnt] "+s"(cnt), [last] "+s"(last), [a0] "+v"(a0), [a1] "+v"(a1), [a2] "+v"(a2), [a3] "+v"(a3)
    : [o] "v"(off), [r] "s"(rs), [la] "v"(maddr), [fa] "v"(faddr), [want] "s"(want), [s1] "s"(8192), [s2] "s"(16384), [s3] "s"(24576), [one] "n"(ONE), [fl] "n"(FL), [gs] "n"(GSLEEP)
    : "memory", "scc", "s90", "s91", "s92", "s93", "s94", "s95");
}
__device__ __forceinline__ u32 or4(v4u v) { return (v.x | v.y | v.z) | v.w; }
template <int STEP, int SI> __device__ __forceinline__ void settle(LAS char* L, int lane, __amdgpu_buffer_rsrc_t rs, int off, u32 tg, v4u& a0, v4u& a1, v4u& a2, v4u& a3) {
  a0 ^= tg; a1 ^= tg; a2 ^= tg; a3 ^= tg;
  if (__builtin_expect(__any((((or4(a0) | or4(a1)) | (or4(a2) | or4(a3))) & 0x3FFu) != 0), 0)) {
    STAMP(SI);
    a0 ^= tg; a1 ^= tg; a2 ^= tg; a3 ^= tg;
    _Pragma("clang loop unroll(disable)") for (int spins = 0; spins < SPIN_CAP; ++spins) {
      const u64 i0 = bad4(a0, tg), i1 = bad4(a1, tg), i2 = bad4(a2, tg), i3 = bad4(a3, tg);
      if (!(i0 | i1 | i2 | i3)) break;
      round4<STEP>(rs, off, i0, i1, i2, i3, a0, a1, a2, a3);
    }
    a0 ^= tg; a1 ^= tg; a2 ^= tg; a3 ^= tg;
  }
}
#define GATHER(OFF, MADDR, LAST, FLAG, WANT, FRAGS, ONE, SI) do { \
    u32 last_ = (LAST); \
    __builtin_amdgcn_s_setprio(GPRIO); \
    pgather<1, ONE>(rs, OFF, (u32)(size_t)(MADDR), (u32)(size_t)(FLAG), WANT, last_, a0, a1, a2, a3); \
    lds_wait_ge(FLAG, WANT); \
    FRAGS; \
    pgather<0, ONE>(rs, OFF, (u32)(size_t)(MADDR), 0, 0, last_, a0, a1, a2, a3); \
    if (isWO) { STAMP(22); } else { STAMP(27); } \
    settle<8192, SI>(L, lane, rs, OFF, tg, a0, a1, a2, a3); } while (0)
__device__ __forceinline__ void mark(char* p, u32 bit) { __hip_atomic_fetch_xor((GAS u32*)p, bit, __ATOMIC_RELAXED, MSC ? __HIP_MEMORY_SCOPE_SYSTEM : __HIP_MEMORY_SCOPE_AGENT); }

__global__ void __launch_bounds__(NT) fused_block(Args a) {
  __shared__ __attribute__((aligned(16))) char smem[LDS_BYTES];
  LAS char* L = (LAS char*)smem;
  LAS u32* SY = (LAS u32*)(L + L_SYNC);
  u64 pc0;
  asm volatile("s_getpc_b64 %0" : "=s"(pc0));
  const int tid = threadIdx.x, lane = tid & 63, wave = __builtin_amdgcn_readfirstlane(tid >> 6);
  const int w = blockIdx.x, x8 = w & 7, j = w >> 3;
  const int grp = (j >> 4) * 8 + x8;
  const int slot = grp * 16 + (j & 15);
  const int g = lane >> 4, c = lane & 15;
  const int r8 = lane >> 3, ch = lane & 7;
  const __amdgpu_buffer_rsrc_t rs = mkrsrc(a.ws, WS_END);
  const int mw = WS_FL + (x8 * 32 + (j & (j < 16 ? ~((1 << M1S) - 1) : ~((2 << M2S) - 2)))) * 128;
  if (tid < 96) SY[tid - 32] = tid < 32 ? 0x3F803F80u : 0u;
  if (wave == 0) STAMP(0);
  int wv = wave;
  const u32 swA = (u32)((((ch >> 1) ^ ((r8 >> 1) & 1)) * 2 + (ch & 1)) * 16);
  const u32 swB = (u32)((((ch >> 1) ^ (((r8 >> 1) & 1) | 2)) * 2 + (ch & 1)) * 16);
  const int cb = x8 * 8 + (j & 7), kq = j >> 3;
  v4u xs0, xs1, xf;
  if (KA) asm volatile("" :: "s"(a.wd), "s"(a.out), "s"(a.wq));
  if (wv < 4) {
    __builtin_amdgcn_s_setprio(3);
    const __amdgpu_buffer_rsrc_t rq = mkrsrc(a.wq, DM * 4096 * 2);
    const u32 vo = (u32)(r8 * 8192 + cb * 128);
    dma_run2<1>(rq, vo + swA, vo + swB, (u32)((128 * kq + 32 * wv) * 8192), 8 * 8192, (u32)(size_t)(L + L_WQ) + wv * 4096, 1024, 2);
    xs0 = *(const GAS v4u*)(a.x + lane * 8);
    xs1 = *(const GAS v4u*)(a.x + DM + lane * 8);
    xf = *(const GAS v4u*)(a.x + (c & 1) * DM + 128 * kq + 32 * wv + 8 * g);
    __builtin_amdgcn_sched_barrier(0);
  }
  OPAQUE(wv);
  u64 E0 = 0;
  u32 B0 = 0;
  if (wv == 8) {
    __builtin_amdgcn_s_setprio(3);
    E0 = __hip_atomic_load((GAS u64*)(a.ws + WS_EPOCH), __ATOMIC_RELAXED, __HIP_MEMORY_SCOPE_AGENT);
    B0 = __builtin_amdgcn_raw_buffer_load_b32(rs, lane == 0 ? mw : lane < 5 ? (j < 8 ? WS_M3 + (((x8 * 8 + j) >> M3S) * 4 + lane - 1) * 128 : OOB) : (EX2 && lane < 7 && j < 16) ? WS_CT + ((x8 * 2 + (j >> 3)) * 2 + lane - 5) * 128 : OOB, 0, AUX_SC1);
    __builtin_amdgcn_sched_barrier(0);
  }
  OPAQUE(wv);
  if (wv > 8 && (CPF == 0 || wv < 9 + CPF)) {
    const char* cp = (const char*)(pc0 & ~(u64)255) + (CPF ? (j * CPF + wv - 9) % CODE_KB : min((j & 3) * 7 + (wv - 9), CODE_KB)) * 1024 + lane * 16;
    if (!CPF && cp + 16 > (const char*)(pc0 & ~(u64)255) + CODE_LEN) cp -= 1024;
    glds16(cp, (u32)(size_t)(L + L_DUMMY));
  }
  if (VPT && wv == 15) {
    const u16* tp = lane < 2 ? a.v + ((size_t)slot * 512 + lane * 511) * DH : lane < 4 ? a.wo + (x8 * 512 + (lane & 1) * 511) * DM : lane < 6 ? a.wg + (lane & 1) * (DM * FF - 2) : lane < 8 ? a.wu + (lane & 1) * (DM * FF - 2)
                    : lane < 10 ? a.wd + (lane & 1) * (DM * FF - 2) : a.out;
    u32 keep;
    if (lane < VPT) asm volatile("s_mov_b32 %0, m0\n\ts_mov_b32 m0, %2\n\ts_nop 0\n\tglobal_load_lds_dword %1, off\n\ts_mov_b32 m0, %0" : "=&s"(keep) : "v"(tp), "s"((u32)(size_t)(L + L_DUMMY)) : "memory");
  }
  OPAQUE(wv);
  const __amdgpu_buffer_rsrc_t rk = mkrsrc(a.k + (size_t)slot * 512 * DH, 131072);
  const u32 kvo = (u32)(g * 256 + ((c ^ ((wv & 3) | (g << 2))) * 16));
  if (KPRE && wv >= 4 && wv < 8) {
    __builtin_amdgcn_s_setprio(2);
    kloop(rk, kvo, (u32)(wv - 4) * 1024u, (u32)(size_t)(L + L_KIMG) + (u32)(wv - 4) * 1024u, KPRE);
  }
  u32 kt0 = 0, kt1 = 0;
  if (SPT & 1) {
    const u16* kp = a.k + (size_t)slot * 512 * DH;
    __builtin_amdgcn_sched_barrier(0);
    LGKM0();
    if (SPT & 16) { if ((wv & ~3) == 4) asm volatile("s_load_dword %0, %1, 0\n\ts_load_dword %0, %1, 0x1000\n\ts_load_dword %0, %1, 0x2000\n\ts_load_dword %0, %1, 0x3000\n\ts_load_dword %0, %1, 0x4000\n\ts_load_dword %0, %1, 0x5000\n\ts_load_dword %0, %1, 0x6000\n\ts_load_dword %0, %1, 0x7000" : "=&s"(kt0) : "s"(kp + (wv & 3) * 16384) : "memory"); }
    else if ((SPT & 256) && wv == 5) asm volatile("s_load_dword %0, %2, 0\n\ts_load_dword %1, %3, 0x800" : "=&s"(kt0), "=&s"(kt1) : "s"(a.x), "s"(a.ws) : "memory");
    else if ((SPT & 1024) && wv == 6) asm volatile("s_load_dword %0, %2, 0\n\ts_load_dword %1, %2, 0xfe000" : "=&s"(kt0), "=&s"(kt1) : "s"(a.wq + 128 * kq * 4096 + cb * 64) : "memory");
    else if ((SPT & 2048) && wv == 7) asm volatile("s_load_dword %0, %2, 0\n\ts_load_dword %1, %3, 0" : "=&s"(kt0), "=&s"(kt1) : "s"(a.out), "s"(a.wd) : "memory");
    else if ((SPT & 512) && wv == 6) asm volatile("s_load_dword %0, %2, 0\n\ts_load_dword %1, %2, 0x1fffc" : "=&s"(kt0), "=&s"(kt1) : "s"(a.v + (size_t)slot * 512 * DH) : "memory");
    else if (wv == 4) asm volatile("s_load_dword %0, %2, 0\n\ts_load_dword %1, %2, 0x1fffc" : "=&s"(kt0), "=&s"(kt1) : "s"(kp) : "memory");
    __builtin_amdgcn_s_barrier();
    CBAR();
    __builtin_amdgcn_sched_barrier(0);
  } else wg_barrier();
  OPAQUE(wv);
  if (wv >= 4 && wv < 8) {
    const int lam = wv - 4;
    __builtin_amdgcn_s_setprio(2);
    kloop(rk, kvo, (u32)lam * 1024u + KPRE * 4096u, (u32)(size_t)(L + L_KIMG) + (u32)lam * 1024u + KPRE * 4096u, 32 - KPRE);
    __builtin_amdgcn_s_setprio(3);
    lds_set(SY + F_K + lam, lane, 1);
    asm volatile("s_waitcnt vmcnt(%0)" :: "n"(KD / 2) : "memory");
    lds_set(SY + F_K + lam, lane, 2);
    VM0();
    lds_set(SY + F_K + lam, lane, 3);
    lds_add(SY + C_KL, lane);
    asm volatile("s_waitcnt lgkmcnt(0)" :: "s"(kt0), "s"(kt1) : "memory");
    STAMP(12);
  }
  OPAQUE(wv);
  if (wv == 8) {
    asm volatile("" : "+v"(E0), "+v"(B0));
    if (lane == 0) *(LAS u64*)(SY + W_E) = E0 + 1;
    if (lane < 7) SY[W_B + lane] = B0;
    lds_set(SY + F_E, lane, 1);
    STAMP(19);
  }
  OPAQUE(wv);
  if (wv < 4) {
    float m0_, m1_, d0_, d1_;
    {
      float s0 = 0.f, s1 = 0.f;
#pragma unroll
      for (int e = 0; e < 4; e++) { s0 += bflo(xs0[e]) + bfhi(xs0[e]); s1 += bflo(xs1[e]) + bfhi(xs1[e]); }
      s0 = wave_sum(s0); s1 = wave_sum(s1);
      const float mean0 = s0 * (1.0f / DM), mean1 = s1 * (1.0f / DM);
      float q0 = 0.f, q1 = 0.f;
#pragma unroll
      for (int e = 0; e < 4; e++) {
        float t;
        t = bflo(xs0[e]) - mean0; q0 = fmaf(t, t, q0);
        t = bfhi(xs0[e]) - mean0; q0 = fmaf(t, t, q0);
        t = bflo(xs1[e]) - mean1; q1 = fmaf(t, t, q1);
        t = bfhi(xs1[e]) - mean1; q1 = fmaf(t, t, q1);
      }
      q0 = wave_sum(q0); q1 = wave_sum(q1);
      m0_ = mean0; m1_ = mean1; d0_ = q0; d1_ = q1;
    }
    const float mean = (c & 1) ? m1_ : m0_, qq = (c & 1) ? d1_ : d0_;
    const float m = rbf(mean), vv = rbf(qq * (1.0f / DM));
    const float den = rbf(sqrtf(rbf(vv + 1e-5f)));
    v4u af;
#pragma unroll
    for (int e = 0; e < 4; e++) af[e] = cvtpk(rbf(bflo(xf[e]) - m) / den, rbf(bfhi(xf[e]) - m) / den);
    STAMP(20);
    VM0();
    STAMP(18);
    {
      const int r0_ = 8 * g + (c >> 2);
      const int sg0 = ((r0_ >> 1) & 1) | ((g & 1) << 1);
      LAS char* bp = L + L_WQ + wv * 4096 + r0_ * 128 + (c & 3) * 8;
      f4 d[4];
#pragma unroll
      for (int t = 0; t < 4; t++) {
        v2u b0 = trread(bp + ((t ^ sg0) * 32)), b1 = trread(bp + 512 + ((t ^ sg0) * 32));
        d[t] = mfma16(af, v4u{b0.x, b0.y, b1.x, b1.y}, f4{0.f, 0.f, 0.f, 0.f});
      }
      const f4 dd = (g & 1) ? f4{d[0][1], d[1][1], d[2][1], d[3][1]} : f4{d[0][0], d[1][0], d[2][0], d[3][0]};
      if (lane < 32) *(LAS f4*)(L + L_QPART + wv * 512 + lane * 16) = dd;
    }
    lds_wait_ge(SY + F_E, 1);
    const u32 tg = SY[W_E];
    if (lds_add_ret(SY + C_QP, lane) == 3) {
      const int l32 = lane & 31;
      f4 sq = *(LAS f4*)(L + L_QPART + l32 * 16);
#pragma unroll
      for (int uu = 1; uu < 4; uu++) sq += *(LAS f4*)(L + L_QPART + uu * 512 + l32 * 16);
      if (Q4) {
        const u32 t8 = tg & 0xFFu;
        v4u qe;
#pragma unroll
        for (int e = 0; e < 4; e++) qe[e] = ((__float_as_uint(sq[e]) + 0x80u) & ~0xFFu) | t8;
        __builtin_amdgcn_raw_buffer_store_b128(qe, rs, lane < 32 ? QADDR(((g * 8 + x8) * 4 + ((j & 7) >> 1)) * 2048 + kq * 512 + (j & 1) * 256 + c * 16) : OOB, 0, AUX_SC1);
      } else {
      const int qo = QADDR(((((g & 1) * 8 + x8) * 4 + ((j & 7) >> 1)) * 4 + kq) * 1024 + (j & 1) * 512 + (g >> 1) * 256 + c * 16);
      __builtin_amdgcn_raw_buffer_store_b128(v4u{__float_as_uint(g & 2 ? sq[2] : sq[0]), tg, __float_as_uint(g & 2 ? sq[3] : sq[1]), tg}, rs, qo, 0, AUX_SC1);
      }
      STAMP(1);
    }
    if (Q4) {
      const int qoff = QADDR((grp * 4 + wv) * 2048 + lane * 16);
      const u32 t8 = tg & 0xFFu;
      __builtin_amdgcn_s_sleep(QSLEEP);
      v4u q0, q1;
      _Pragma("clang loop unroll(disable)") for (int spins = 0; spins < SPIN_CAP; ++spins) {
        CBAR();
        q0 = ldx(rs, qoff, t8); q1 = ldx(rs, qoff + 1024, t8);
        if (__all(((or4(q0) | or4(q1)) & 0xFFu) == 0)) break;
      }
      const f4 qs = F4(q0) + F4(q1);
      auto pa = __builtin_amdgcn_permlane32_swap(__float_as_uint(qs[0]), __float_as_uint(qs[2]), false, false);
      auto pb = __builtin_amdgcn_permlane32_swap(__float_as_uint(qs[1]), __float_as_uint(qs[3]), false, false);
      const u32 pk = cvtpk(__uint_as_float(pa[0]) + __uint_as_float(pa[1]), __uint_as_float(pb[0]) + __uint_as_float(pb[1]));
      LAS char* qp = L + L_QS + wv * 256 + ((g & 1) * 64 + (g >> 1) * 32 + c) * 2;
      *(LAS u16*)qp = (u16)pk;
      *(LAS u16*)(qp + 32) = (u16)(pk >> 16);
      lds_add(SY + C_Q, lane);
      STAMP(29);
    } else {
      const int qoff = QADDR((grp * 4 + wv) * 4096 + lane * 16);
      __builtin_amdgcn_s_sleep(QSLEEP);
      v4u qg[4];
      _Pragma("clang loop unroll(disable)") for (int spins = 0; spins < SPIN_CAP; ++spins) {
        CBAR();
#pragma unroll
        for (int q = 0; q < 4; q++) qg[q] = __builtin_amdgcn_raw_buffer_load_b128(rs, qoff + q * 1024, 0, AUX_SC1);
        bool ok = true;
#pragma unroll
        for (int q = 0; q < 4; q++) ok = ok & (qg[q].y == tg) & (qg[q].w == tg);
        if (__all(ok)) break;
      }
      const float qa = (__uint_as_float(qg[0].x) + __uint_as_float(qg[1].x)) + (__uint_as_float(qg[2].x) + __uint_as_float(qg[3].x));
      const float qb = (__uint_as_float(qg[0].z) + __uint_as_float(qg[1].z)) + (__uint_as_float(qg[2].z) + __uint_as_float(qg[3].z));
      const u32 pk = cvtpk(qa, qb);
      LAS char* qp = L + L_QS + wv * 256 + (g * 32 + c) * 2;
      *(LAS u16*)qp = (u16)pk;
      *(LAS u16*)(qp + 32) = (u16)(pk >> 16);
      lds_add(SY + C_Q, lane);
      STAMP(29);
    }
  }
  const bool isWO = j < 16;
  const int hw = (j >> 3) & 1, wi = 2 * x8 + hw, wjj = j & 7;
  const int fidx = j - 16;
  const bool isFFN = (j >= 16) && (x8 < 4 || fidx < 12);
  const int fblk0 = x8 < 4 ? x8 * 4 : 4 + x8 * 3;
  const int ff = (fblk0 + (fidx >> 2)) * 2 + ((fidx >> 1) & 1);
  const int fb = fidx & 1;
  OPAQUE(wv);
  if (wv >= 8) {
    const int u = wv - 8;
    __builtin_amdgcn_s_setprio(1);
    v4u V[16];
    {
      const __amdgpu_buffer_rsrc_t rv = mkrsrc(a.v + (size_t)slot * 512 * DH, 131072);
      const int vo = u * 16384 + lane * 16;
      if ((SPT & 32) && u >= 4) {
        __builtin_amdgcn_s_sleep(SPD);
        u32 t0;
        asm volatile("s_load_dword %0, %1, 0\n\ts_load_dword %0, %1, 0x1000\n\ts_load_dword %0, %1, 0x2000\n\ts_load_dword %0, %1, 0x3000\n\ts_load_dword %0, %1, 0x4000\n\ts_load_dword %0, %1, 0x5000\n\ts_load_dword %0, %1, 0x6000\n\ts_load_dword %0, %1, 0x7000" "\n\ts_waitcnt lgkmcnt(0)" : "=&s"(t0) : "s"(a.v + (size_t)slot * 512 * DH + (u & 3) * 16384) : "memory");
      }
      if ((SPT & 2) && u == 7) {
        __builtin_amdgcn_s_sleep(SPD);
        u32 t0, t1, t2, t3;
        if (!(SPT & 8) || (j & 15) == 0 || (j & 15) == 15)
          asm volatile("s_load_dword %0, %2, 0\n\ts_load_dword %1, %2, 0x1fffc\n\ts_waitcnt lgkmcnt(0)" : "=&s"(t0), "=&s"(t1) : "s"(a.v + (size_t)slot * 512 * DH) : "memory");
        if (!(SPT & 4) || j == 7)
          asm volatile("s_load_dword %2, %4, 0\n\ts_load_dword %3, %4, 0x7fffc\n\ts_waitcnt lgkmcnt(0)\n\t"
                     "s_load_dword %0, %5, 0\n\ts_load_dword %1, %5, %9\n\ts_load_dword %2, %6, 0\n\ts_load_dword %3, %6, %9\n\ts_waitcnt lgkmcnt(0)\n\t"
                     "s_load_dword %0, %7, 0\n\ts_load_dword %1, %7, %9\n\ts_load_dword %2, %8, 0\n\ts_waitcnt lgkmcnt(0)"
                     : "=&s"(t0), "=&s"(t1), "=&s"(t2), "=&s"(t3)
                     : "s"(a.wo + x8 * 512 * DM), "s"(a.wg), "s"(a.wu), "s"(a.wd), "s"(a.out), "s"(DM * FF * 2 - 4) : "memory");
      }
      if (VP2 && u == 7) {
        __builtin_amdgcn_s_sleep(VP2);
        u32 keep;
        asm volatile("s_mov_b32 %[keep], m0\n\ts_mov_b32 m0, %[l]\n\ts_nop 0\n\tbuffer_load_dword %[v], %[r], 0 offen lds\n\ts_mov_b32 m0, %[keep]"
                     : [keep] "=&s"(keep) : [v] "v"(lane * 4096), [r] "s"(rv), [l] "s"((u32)(size_t)(L + L_DUMMY)) : "memory");
      }
#pragma unroll
      for (int hf = 0; hf < 2; hf++) {
        const int n = 2 * u + hf;
        if (n < VDU) lds_wait_ge(SY + F_K + (n & 3), KREL); else lds_wait_ge(SY + C_VL, n - VDU + 1);
#pragma unroll
        for (int t = 0; t < 8; t++) V[8 * hf + t] = __builtin_amdgcn_raw_buffer_load_b128(rv, vo, (8 * hf + t) * 1024, 2);
        __builtin_amdgcn_sched_barrier(0);
      }
    }
    __builtin_amdgcn_s_setprio(3);
    lds_add(SY + C_VISS, lane);
    STAMP(2);
    if (u < 6) {
      asm volatile("s_waitcnt vmcnt(8)" ::: "memory");
      lds_add(SY + C_VL, lane);
      VM0();
      lds_add(SY + C_VL, lane);
    }
    lds_wait_ge(SY + F_P + u, 1);
    const u32 tg = SY[W_E] & 0x3FFu;
    char* mk1p = a.ws + WS_FL + (x8 * 32 + (c << M1S)) * 128;
    u32 mb1 = 1u << j;
    const int mkl = u == 7 ? 16 >> M1S : 0;
    int so1 = WS_MB1 + slot * 2048 + lane * 16;
    u32 arr = 0, aar_ = (u32)(size_t)(SY + (u >= 6 ? C_A : C_X)), amw = (u32)(size_t)L + (u == 7 ? L_QPART : L_MRG + u * 2048) + lane * 16, amr = (u32)(size_t)L + (u == 6 ? L_QPART : L_MRG + 6 * 2048) + lane * 16;
    PIN(mk1p); PIN(mb1); PIN(so1); PIN(aar_); PIN(amw); PIN(amr);
    f4 s0 = {0.f, 0.f, 0.f, 0.f}, s1 = {0.f, 0.f, 0.f, 0.f};
#pragma unroll
    for (int G = 0; G < 2; G++) {
      const v4u pf = *(LAS v4u*)(L + L_PFR + ((u * 2 + G) * 16 + g * 4 + (c & 3)) * 16);
      v4u pm[4];
#pragma unroll
      for (int d2 = 0; d2 < 4; d2++) pm[d2] = (c >> 2) == d2 ? pf : v4u{0u, 0u, 0u, 0u};
      asm volatile("" : "+v"(pm[0]), "+v"(pm[1]), "+v"(pm[2]), "+v"(pm[3]));
      if (G == 1 && u == 6) {
        lds_add(SY + C_VL, lane);
        lds_wait_ge(SY + C_O, 6);
#pragma unroll
        for (int uu = 0; uu < 6; uu++) { s0 += *(LAS f4*)(L + L_MRG + uu * 2048 + lane * 16); s1 += *(LAS f4*)(L + L_MRG + uu * 2048 + 1024 + lane * 16); }
        if (M1W != 4) { VM0(); lds_add(SY + C_VL, lane); }
        asm volatile("" : "+v"(s0), "+v"(s1));
      }
      if (M1W >= 3 && G == 1) {
        if (u == 7) lds_add(SY + C_VL, lane);
        v4u lo[4], hi[4];
#pragma unroll
        for (int t2 = 0; t2 < 4; t2++) {
          __builtin_amdgcn_sched_barrier(0);
          if (M1W == 3 && t2 == PFOLD && u == 7) {
            lds_spin_ge(SY + C_O, 7);
            s0 += *(LAS f4*)(L + L_MRG + 6 * 2048 + lane * 16); s1 += *(LAS f4*)(L + L_MRG + 6 * 2048 + 1024 + lane * 16);
            asm volatile("" : "+v"(s0), "+v"(s1));
            __builtin_amdgcn_sched_barrier(0);
          }
          if (M1W == 4 && t2 == 3) {
            __builtin_amdgcn_s_waitcnt(0x0F70);
            u64 keep;
            asm volatile("s_mov_b64 %0, exec\n\ts_mov_b64 exec, 1\n\tds_add_rtn_u32 %1, %2, %3\n\ts_mov_b64 exec, %0" : "=&s"(keep), "=&v"(arr) : "v"(aar_), "v"(1u) : "memory");
            __builtin_amdgcn_sched_barrier(0);
          }
          if (MKE && t2 == 3) {
            __builtin_amdgcn_s_waitcnt(0x0F70);
            __builtin_amdgcn_sched_barrier(0);
            if (lane < mkl) mark(mk1p, mb1);
            __builtin_amdgcn_sched_barrier(0);
          }
#pragma unroll
          for (int d2 = 0; d2 < 4; d2++) {
            const u32 va = V[8 + 2 * t2][d2], vb = V[8 + 2 * t2 + 1][d2];
            u32 l_ = __builtin_amdgcn_perm(vb, va, 0x05040100u), h_ = __builtin_amdgcn_perm(vb, va, 0x07060302u);
            if (t2 < 3) asm volatile("" : "+v"(l_), "+v"(h_));
            lo[d2][t2] = l_; hi[d2][t2] = h_;
            if (t2 == 3) { s0 = mfma16(pm[d2], lo[d2], s0); s1 = mfma16(pm[d2], hi[d2], s1); }
          }
        }
        __builtin_amdgcn_sched_barrier(0);
        break;
      }
      if (G == 1) {
        char* mk1 = a.ws + WS_FL + (x8 * 32 + c) * 128;
        if (u == 7) {
          lds_add(SY + C_VL, lane);
          if (M1W == 1) {
            lds_spin_ge(SY + C_O, 7);
            s0 += *(LAS f4*)(L + L_MRG + 6 * 2048 + lane * 16); s1 += *(LAS f4*)(L + L_MRG + 6 * 2048 + 1024 + lane * 16);
          }
        }
        asm volatile("" : "+v"(s0), "+v"(s1), "+v"(mk1));
        __builtin_amdgcn_sched_barrier(0);
        __builtin_amdgcn_s_waitcnt(0x0F70);
        __builtin_amdgcn_sched_barrier(0);
        if (u == 7) {
          if (M1W == 1) { if (lane < 16) mark(mk1, 1u << j); }
          else if (M1W == 0) lds_add(SY + C_VL, lane);
        }
      }
      __builtin_amdgcn_sched_barrier(0);
#pragma unroll
      for (int d2 = 0; d2 < 4; d2++) {
        v4u lo, hi;
#pragma unroll
        for (int t2 = 0; t2 < 4; t2++) {
          const u32 va = V[8 * G + 2 * t2][d2], vb = V[8 * G + 2 * t2 + 1][d2];
          lo[t2] = __builtin_amdgcn_perm(vb, va, 0x05040100u);
          hi[t2] = __builtin_amdgcn_perm(vb, va, 0x07060302u);
        }
        s0 = mfma16(pm[d2], lo, s0);
        s1 = mfma16(pm[d2], hi, s1);
      }
      __builtin_amdgcn_sched_barrier(0);
    }
    bool pub = u == 7;
    if (M1W == 4) {
      asm volatile("s_waitcnt lgkmcnt(0)" : "+v"(arr) :: "memory");
      if (u >= 6 && __builtin_amdgcn_readfirstlane(arr) == 1 && lane < (16 >> M1S)) mark(mk1p, mb1);
      __builtin_amdgcn_sched_barrier(0);
      *LP(f4, amw) = s0;
      *LP(f4, amw + 1024) = s1;
      pub = false;
      if (u < 6) lds_add(SY + C_O, lane);
      else {
        u64 keep;
        f4 o0, o1;
        asm volatile("s_mov_b64 %0, exec\n\ts_mov_b64 exec, 1\n\tds_add_rtn_u32 %1, %4, %5 offset:4\n\ts_mov_b64 exec, %0\n\t"
                     "ds_read_b128 %2, %6\n\tds_read_b128 %3, %6 offset:1024\n\ts_waitcnt lgkmcnt(0)"
                     : "=&s"(keep), "=&v"(arr), "=&v"(o0), "=&v"(o1) : "v"(aar_), "v"(1u), "v"(amr) : "memory");
        pub = __builtin_amdgcn_readfirstlane(arr) == 1;
        s0 += o0; s1 += o1;
      }
    } else if (u < 7) {
      *(LAS f4*)(L + L_MRG + u * 2048 + lane * 16) = s0;
      *(LAS f4*)(L + L_MRG + u * 2048 + 1024 + lane * 16) = s1;
      lds_add(SY + C_O, lane);
    }
    if (pub) {
      if (M1W == 3) { if (!MKE && lane < (16 >> M1S)) mark(mk1p, mb1); __builtin_amdgcn_sched_barrier(0); }
      else if (M1W != 1 && M1W != 4) {
        lds_spin_ge(SY + C_O, 7);
        if (M1W == 2 && lane < (16 >> M1S)) mark(a.ws + WS_FL + (x8 * 32 + (c << M1S)) * 128, 1u << j);
        s0 += *(LAS f4*)(L + L_MRG + 6 * 2048 + lane * 16); s1 += *(LAS f4*)(L + L_MRG + 6 * 2048 + 1024 + lane * 16);
      }
      const v4u e0 = enc4(s0, tg), e1 = enc4(s1, tg);
      __builtin_amdgcn_raw_buffer_store_b128(e0, rs, so1, 0, AUX_SC1);
      __builtin_amdgcn_raw_buffer_store_b128(e1, rs, so1 + 1024, 0, AUX_SC1);
      *(LAS v4u*)(L + L_MRG + 7 * 2048 + lane * 16) = hw ? e1 : e0;
      lds_add(SY + C_PUB, lane);
      STAMP(4);
    }
    const int r0_ = 8 * g + (c >> 2);
    if (isWO) {
      const int bq = u >> 2, qd = u & 3;
      const int off = WS_MB1 + ((bq * 8 + x8) * 16 + g) * 2048 + hw * 1024 + qd * 256 + c * 16;
      f4 wt[4];
      const u32 Lb = (u32)(size_t)L;
      u32 aow = Lb + L_OB + u * 128 + (c * 4 + g) * 2, aor = Lb + L_OB + u * 128 + g * 16, apw = Lb + L_WP + u * 256 + c * 16, apr = Lb + L_WP + bq * 1024 + c * 16;
      u32 act_ = (u32)(size_t)(SY + C_WO + bq);
      int so2 = lane < 16 ? WS_MB2 + ((bq * 16 + wi) * 8 + wjj) * 256 + c * 16 : OOB;
      PIN(aow); PIN(aor); PIN(apw); PIN(apr); PIN(act_); PIN(so2);
      lds_wait_ge(SY + C_PUB, 1);
      v4u a0, a1, a2, a3, bf[8];
      const int sg0 = ((r0_ >> 1) & 1) | ((g & 1) << 1);
      LAS char* bp = L + L_WO + (qd * 64 + r0_) * 128 + (c & 3) * 8;
#define WOFRAGS _Pragma("unroll") for (int i = 0; i < 8; i++) { LAS char* p = bp + (i >> 2) * 4096 + (((i & 3) ^ sg0) * 32); \
        v2u t0 = trread(p), t1 = trread(p + 512); bf[i] = v4u{t0.x, t0.y, t1.x, t1.y}; } \
        pgather<1, ONE1>(rs, off, (u32)(size_t)((LAS char*)(SY + F_GO) + bq * 2), (u32)(size_t)(SY + F_WT), 1, last_, a0, a1, a2, a3); \
        lds_wait_ge(SY + F_WT, 1); \
        _Pragma("unroll") for (int i = 0; i < 4; i++) wt[i] = *(LAS f4*)(L + L_WT + (bq * 16 + g + 4 * i) * 16);
      {
        const v4u own = *(LAS v4u*)(L + L_MRG + 7 * 2048 + qd * 256 + c * 16), bad = v4u{~tg, ~tg, ~tg, ~tg};
        const int d = bq ? -1 : j - g;
        a0 = d == 0 ? own : bad; a1 = d == 4 ? own : bad; a2 = d == 8 ? own : bad; a3 = d == 12 ? own : bad;
      }
      GATHER(off, (LAS char*)(SY + F_GO) + bq * 2, bq ? 0u : 1u << j, SY + C_W, 4, WOFRAGS, ONE1, 23);
      if (EX2 == 1) lds_add(SY + C_G + bq, lane);
      const float o = rscatter((F4(a0) * wt[0] + F4(a1) * wt[1]) + (F4(a2) * wt[2] + F4(a3) * wt[3]));
      STAMP(5);
      *LP(u16, aow) = (u16)cvtpk(o, o);
      CBAR();
      const v4u of0 = *LP(v4u, aor), of1 = *LP(v4u, aor + 64);
      f4 d[4];
#pragma unroll
      for (int t = 0; t < 4; t++) d[t] = mfma16(of0, bf[t], f4{0.f, 0.f, 0.f, 0.f});
#pragma unroll
      for (int t = 0; t < 4; t++) d[t] = mfma16(of1, bf[4 + t], d[t]);
      *LP(f4, apw) = f4{d[0][0], d[1][0], d[2][0], d[3][0]};
      f4 p0, p1, p2, p3;
      if (lds_add_read4(act_, apr, p0, p1, p2, p3) == 3) {
        __builtin_amdgcn_raw_buffer_store_b128(enc4((p0 + p1) + (p2 + p3), tg), rs, so2, 0, AUX_SC1);
        lds_add(SY + C_PUB2, lane);
        STAMP(16);
      }
    } else if (isFFN) {
      const u32 xv = *(const GAS u16*)(a.x + fb * DM + u * 64 + lane);
      const int off = WS_MB2 + ((fb * 16 + g) * 8 + u) * 256 + c * 16;
      v4u a0, a1, a2, a3, bg[8], bd[P4 ? 8 : 4];
      const u32 Lb = (u32)(size_t)L;
      u32 ahw = Lb + L_OB + u * 128 + lane * 2, ahr = Lb + ((c & 1) ? L_ONES : L_OB + u * 128 + g * 16);
      u32 asw = Lb + L_STAT + u * 8, apw = Lb + (lane < 32 ? L_WP + g * 2048 + u * 256 + c * 16 : L_DUMMY + lane * 16);
      u32 asr = Lb + L_STAT + (lane & 7) * 8, apr = Lb + L_WP + g * 512 + c * 16;
      u32 aaw = Lb + L_ACT + u * 128 + lane * 2;
      u32 aar = (P4 && c >= 4) ? (u32)(size_t)(SY + W_Z) : Lb + L_ACT + u * 128 + g * 16, aar2 = (c >> 2) != 1 ? (u32)(size_t)(SY + W_Z) : Lb + L_ACT + u * 128 + g * 16;
      const int t0_ = (r0_ & 3) | ((g & 1) << 2);
      u32 adn[P4 ? 8 : 4];
#pragma unroll
      for (int tt = 0; tt < (P4 ? 8 : 4); tt++) { adn[tt] = Lb + L_DN + r0_ * 1024 + ((((P4 ? u * 8 : u * 4) + tt) ^ t0_) * 32) + (c & 3) * 8; PIN(adn[tt]); }
      char* mk3 = a.ws + WS_M3 + ((((fb * 32 + (P4 ? u * 8 : u * 4)) >> M3S) + (lane & 7)) * 4 + (ff & 3)) * 128;
      u32 mb3 = 1u << (ff >> 2);
      int so3 = lane < (P4 ? 32 : 16) ? WS_MB3 + (((fb * 8 + (P4 ? 2 * u + g : u)) * 2 + (c >> 3)) * 56 + ff) * 128 + (c & 7) * 16 : OOB;
      PIN(ahw); PIN(ahr); PIN(asw); PIN(apw); PIN(asr); PIN(apr); PIN(aaw); PIN(aar); if (P4) PIN(aar2); PIN(mk3); PIN(mb3); PIN(so3);
      LAS char* bp = L + L_GU + (u * 64 + r0_) * 64 + (c & 3) * 8;
#define GUFRAGS _Pragma("unroll") for (int i = 0; i < 8; i++) { LAS char* p = bp + (i >> 2) * 2048 + ((i >> 1) & 1) * 32768 + (((i & 1) ^ (g & 1)) * 32); \
        v2u t0 = trread(p), t1 = trread(p + 256); bg[i] = v4u{t0.x, t0.y, t1.x, t1.y}; }
      a0 = a1 = a2 = a3 = v4u{~tg, ~tg, ~tg, ~tg};
      GATHER(off, SY + F_GO, 0u, SY + F_K + (u >> 1), 5, GUFRAGS, ONE2, 26);
      const float at = rscatter((F4(a0) + F4(a1)) + (F4(a2) + F4(a3)));
      const float hs = bflo(xv) + rbf(at);
      const u32 hb = cvtpk(hs, hs);
      STAMP(6);
      *LP(u16, ahw) = (u16)hb;
      CBAR();
      const v4u hf0 = *LP(v4u, ahr), hf1 = *LP(v4u, ahr + 64);
      const float hv = bflo(hb);
      const float su = wave_sum(hv);
      f4 d[4];
#pragma unroll
      for (int t = 0; t < 4; t++) d[t] = mfma16(hf0, bg[t], f4{0.f, 0.f, 0.f, 0.f});
      const float dv = hv - su * (1.0f / 64);
      const float m2 = wave_sum(dv * dv);
#pragma unroll
      for (int t = 0; t < 4; t++) d[t] = mfma16(hf1, bg[4 + t], d[t]);
      *LP(f2, asw) = f2{su, m2};
      *LP(f4, apw) = g ? f4{d[0][1], d[1][1], d[2][1], d[3][1]} : f4{d[0][0], d[1][0], d[2][0], d[3][0]};
      wg_barrier();
      STAMP(24);
      if (!P4 || u < 4) {
#define MARK3(P) do { if (M3POS == (P)) { __builtin_amdgcn_sched_barrier(0); if (lane < ((P4 ? 8 : 4) >> M3S)) mark(mk3, mb3); __builtin_amdgcn_sched_barrier(0); } } while (0)
      MARK3(0);
      const f2 stt = *LP(f2, asr);
      const f4 pa = *LP(f4, apr), pb = *LP(f4, apr + 256), ca = *LP(f4, apr + 2048), cb2 = *LP(f4, apr + 2048 + 256);
#define DNFRAGS() do { \
_Pragma("unroll") \
        for (int tt = 0; tt < (P4 ? 8 : 4); tt++) { \
          const u32 ad = adn[tt]; \
          v2u b0 = trread(LP(char, ad)), b1 = trread(LP(char, ad + 4096)); \
          bd[tt] = v4u{b0.x, b0.y, b1.x, b1.y}; \
        } \
      } while (0)
      if (!LBAR) DNFRAGS();
      __builtin_amdgcn_sched_barrier(0);
      const f4 pp = pa + pb;
      const float colsum = rscatter(ca + cb2);
      float S = stt[0];
      S += dpp<XOR1>(S); S += dpp<XOR2>(S); S += dpp<XOR7>(S);
      const float mean = S * (1.0f / DM);
      const float dm = stt[0] * (1.0f / 64) - mean;
      float Q = fmaf(64.f * dm, dm, stt[1]);
      Q += dpp<XOR1>(Q); Q += dpp<XOR2>(Q); Q += dpp<XOR7>(Q);
      const float dsum = rscatter(pp);
      const float m = rbf(mean), vv = rbf(Q * (1.0f / DM));
      const float den = rbf(__builtin_amdgcn_sqrtf(rbf(vv + 1e-5f)));
      const float pre = rbf((dsum - m * colsum) * __builtin_amdgcn_rcpf(den));
      MARK3(1);
      if (LBAR) { lds_spin_ge(SY + C_W, 4); DNFRAGS(); }
      auto sw = __builtin_amdgcn_permlane32_swap(__float_as_uint(pre), __float_as_uint(pre), false, false);
      const float upv = __uint_as_float(sw[1]);
      const float sg = rbf(pre * __builtin_amdgcn_rcpf(1.0f + __expf(-pre)));
      const float act = sg * upv;
      *LP(u16, aaw) = (u16)cvtpk(act, act);
      CBAR();
      const v4u af = *LP(v4u, aar), af2 = P4 ? *LP(v4u, aar2) : af;
      MARK3(2);
      f4 o[4];
#pragma unroll
      for (int tt = 0; tt < 4; tt++) o[tt] = mfma16(af, bd[tt], f4{0.f, 0.f, 0.f, 0.f});
      __builtin_amdgcn_sched_barrier(0);
      if (P4) {
#pragma unroll
        for (int tt = 0; tt < 4; tt++) o[tt] = mfma16(af2, bd[(4 + tt) % (P4 ? 8 : 4)], o[tt]);
      }
      __builtin_amdgcn_sched_barrier(0);
      __builtin_amdgcn_raw_buffer_store_b128(enc4(f4{o[0][0], o[1][0], o[2][0], o[3][0]}, tg), rs, so3, 0, AUX_SC1);
      MARK3(3);
      STAMP(8);
      }
    }
  }
  OPAQUE(wv);
  if (wv < 8) {
    const int u = wv;
    lds_wait_ge(SY + C_KL, 4);
    lds_wait_ge(SY + C_Q, 4);
    STAMP(11);
    {
      v4u qf[4];
#pragma unroll
      for (int kk = 0; kk < 4; kk++) qf[kk] = *(LAS v4u*)(L + L_QS + (c & 3) * 256 + (32 * kk + 8 * g) * 2);
      float sc[16];
#pragma unroll
      for (int T = 0; T < 4; T++) {
        f4 d = {0.f, 0.f, 0.f, 0.f};
        const int row = 16 * T + 4 * (c & 3) + (c >> 2);
#pragma unroll
        for (int kk = 0; kk < 4; kk++) {
          const v4u kf = *(LAS v4u*)(L + L_KIMG + (64 * u + row) * 256 + (((4 * kk + g) ^ c) * 16));
          d = mfma16(kf, qf[kk], d);
        }
#pragma unroll
        for (int i = 0; i < 4; i++) sc[4 * T + i] = rbf(rbf(d[i]) * 0.08838834764831845f);
      }
      float m = sc[0];
#pragma unroll
      for (int i = 1; i < 16; i++) m = fmaxf(m, sc[i]);
      m = x16_max(m);
      m = x32_max(m);
      if (lane < 4) *(LAS float*)(L + L_ML + (lane * 8 + u) * 4) = m;
      lds_add(SY + C_M, lane);
      lds_spin_ge(SY + C_M, 8);
      {
        const f4 ma = *(LAS f4*)(L + L_ML + (c & 3) * 32), mb = *(LAS f4*)(L + L_ML + (c & 3) * 32 + 16);
        m = fmaxf(fmaxf(fmaxf(ma[0], ma[1]), fmaxf(ma[2], ma[3])), fmaxf(fmaxf(mb[0], mb[1]), fmaxf(mb[2], mb[3])));
      }
      float l = 0.f;
#pragma unroll
      for (int i = 0; i < 16; i++) { sc[i] = __builtin_amdgcn_exp2f((sc[i] - m) * 1.4426950408889634f); l += sc[i]; }
      if (c < 4) {
#pragma unroll
        for (int G = 0; G < 2; G++) {
          v4u pf = {cvtpk(sc[8 * G], sc[8 * G + 1]), cvtpk(sc[8 * G + 2], sc[8 * G + 3]), cvtpk(sc[8 * G + 4], sc[8 * G + 5]), cvtpk(sc[8 * G + 6], sc[8 * G + 7])};
          *(LAS v4u*)(L + L_PFR + ((u * 2 + G) * 16 + g * 4 + c) * 16) = pf;
        }
      }
      lds_set(SY + F_P + u, lane, 1);
      l = x16_sum(l);
      l = x32_sum(l);
      if (lane < 4) *(LAS float*)(L + L_LS + (lane * 8 + u) * 4) = l;
      STAMP(13);
      lds_add(SY + C_ML, lane);
      if (u == 2) {
        lds_wait_ge(SY + C_ML, 8);
        const f4 la = *(LAS f4*)(L + L_LS + (c & 3) * 32), lb = *(LAS f4*)(L + L_LS + (c & 3) * 32 + 16);
        const float lw = ((la[0] + la[1]) + (la[2] + la[3])) + ((lb[0] + lb[1]) + (lb[2] + lb[3]));
        const v2u e = *(LAS v2u*)(SY + W_E);
        lds_wait_ge(SY + C_VL, SW);
        __builtin_amdgcn_raw_buffer_store_b128(v4u{__float_as_uint(m), __float_as_uint(lw), e.x, e.y}, rs, lane < STP / 16 ? WS_ST + slot * STP + lane * 16 : OOB, 0, AUX_SC1);
        STAMP(14);
      }
    }
    if (!M1W && u == 2) {
      char* mk1 = a.ws + WS_FL + (x8 * 32 + c) * 128;
      lds_wait_ge(SY + C_VL, FLAT - 2);
      lds_spin_ge(SY + C_VL, FLAT);
      if (MKD) __builtin_amdgcn_s_sleep(MKD);
      if (lane < 16) mark(mk1, 1u << j);
    }
    if (C3PF && u >= 4) {
      const int lam = u - 4;
      const u32 dm = (u32)(size_t)(L + L_DUMMY);
      if ((C3PF & 1) && lam < 2) bl_lds(mkrsrc(a.wo, 4096 * DM * 2), (u32)(r8 * 1024 + 3 * 128 + ch * 16), (u32)((x8 * 512 + j * 16 + lam * 8) * 1024), dm);
      if ((C3PF & 2) && x8 != 4) bl_lds(mkrsrc(lam & 1 ? a.wu : a.wg, DM * FF * 2), (u32)(r8 * FF * 2 + (fblk0 | 3) * 128 + ch * 16), (u32)((j * 16 + (lam >> 1) * 8) * FF * 2), dm);
    }
#define PF2() do { \
      const __amdgpu_buffer_rsrc_t pg = mkrsrc(a.wg, DM * FF * 2), pu = mkrsrc(a.wu, DM * FF * 2), pd = mkrsrc(a.wd, FF * DM * 2); \
      const int nl = x8 < 4 ? 4 : 3; \
      const u32 pv = (lane & 31) < nl * 8 ? (u32)((lane >> 5) * FF * 2 + fblk0 * 128 + (lane & 31) * 16) : (u32)OOB; \
      const u32 dm = (u32)(size_t)(L + L_DUMMY); \
      dma_run2<0>(pg, pv, pv, (u32)((16 * j + 4 * lam) * FF * 2), 2 * FF * 2, dm, 0, 1); \
      dma_run2<0>(pu, pv, pv, (u32)((16 * j + 4 * lam) * FF * 2), 2 * FF * 2, dm, 0, 1); \
      if (((PFM & 2) || PFD) && lam < nl) dma_run2<0>(pd, (u32)lane * 16, (u32)lane * 16, (u32)((fblk0 * 64 + (j * nl + lam) * 2) * 1024), 1024, dm, 0, 1); \
    } while (0)
    if (EPF && u >= 4) { const int lam = u - 4; if (EPF > 1) lds_wait_ge(SY + C_VL, EPF); PF2(); }
    if (isWO) {
      if (u == 0) {
        const v2u e = *(LAS v2u*)(SY + W_E);
        lds_wait_ge(SY + C_VL, S0);
        _Pragma("clang loop unroll(disable)") for (int spins = 0; spins < SPIN_CAP; ++spins) {
          if (__builtin_popcount(__builtin_amdgcn_readfirstlane(__hip_atomic_load(SY + F_GO, __ATOMIC_RELAXED, __HIP_MEMORY_SCOPE_WORKGROUP))) >= SN) break;
          __builtin_amdgcn_s_sleep(1);
        }
        const int soff = WS_ST + (x8 * 16 + (lane >> 2)) * STP + (lane & 3) * 16;
        v4u st0, st1;
        _Pragma("clang loop unroll(disable)") for (int spins = 0; spins < SPIN_CAP; ++spins) {
          CBAR();
          st0 = __builtin_amdgcn_raw_buffer_load_b128(rs, soff, 0, AUX_SC1);
          st1 = __builtin_amdgcn_raw_buffer_load_b128(rs, soff + 128 * STP, 0, AUX_SC1);
          if (__all((st0.z == e.x) & (st0.w == e.y) & (st1.z == e.x) & (st1.w == e.y))) break;
          __builtin_amdgcn_s_sleep(STSLEEP);
        }
#pragma unroll
        for (int bb = 0; bb < 2; bb++) {
          const float ms = __uint_as_float(bb ? st1.x : st0.x), ls = __uint_as_float(bb ? st1.y : st0.y);
          float M = ms;
          M = fmaxf(M, x4_get(M)); M = fmaxf(M, dpp<XOR8>(M)); M = x16_max(M); M = x32_max(M);
          const float ex = __builtin_amdgcn_exp2f((ms - M) * 1.4426950408889634f);
          float Ls = ls * ex;
          Ls += x4_get(Ls); Ls += dpp<XOR8>(Ls); Ls = x16_sum(Ls); Ls = x32_sum(Ls);
          *(LAS float*)(L + L_WT + bb * 256 + lane * 4) = ex / Ls;
        }
        lds_set(SY + F_WT, lane, 1);
      }
      if (u == 3) {
        if (M0 > 16) lds_wait_ge(SY + C_PUB, 1); else lds_wait_ge(SY + C_VL, M0);
        watch<0, -1>(a.ws + mw, SY[W_B], (u32)(size_t)(SY + F_GO));
      }
      if (EX2 && (u == 2 || u == 0)) {
        const int bq = u >> 1;
        const u32 base = SY[W_B + 5 + bq];
        char* mk2 = a.ws + WS_FL + (ch * 32 + 16 + 2 * r8 + bq) * 128;
        GAS u32* ct = (GAS u32*)(a.ws + WS_CT + (wi * 2 + bq) * 128);
        lds_wait_ge(SY + C_PUB, 1);
        if (EX2 == 1) lds_wait_ge(SY + C_G + bq, 4);
        else {
          lds_wait_ge(SY + C_W, 4);
          lds_wait_ge(SY + F_WT, 1);
          const u32 ign = bq ? 0xFFFFu : 0xFFFF0000u | (1u << j);
          _Pragma("clang loop unroll(disable)") for (int spins = 0; spins < SPIN_CAP; ++spins) {
            if ((__builtin_amdgcn_readfirstlane(__hip_atomic_load(SY + F_GO, __ATOMIC_RELAXED, __HIP_MEMORY_SCOPE_WORKGROUP)) | ign) == 0xFFFFFFFFu) break;
            __builtin_amdgcn_s_sleep(1);
          }
        }
        STAMP(31);
        if (E2D) __builtin_amdgcn_s_sleep(E2D);
        u32 old = 0;
        if (lane == 0) old = __hip_atomic_fetch_add(ct, 1u, __ATOMIC_RELAXED, __HIP_MEMORY_SCOPE_AGENT);
        if (__builtin_amdgcn_readfirstlane(old - base) == 7) {
          if ((ch < 4 || r8 < 6) && !(r8 & ((1 << M2S) - 1))) mark(mk2, 1u << wi);
          STAMP(25);
        }
      }
      if (u == 1 && j < 8) {
        const u32 tg = SY[W_E] & 0x3FFu;
        const int fi = x8 * 8 + j, bb = fi >> 5, ub = (fi >> 2) & 7, cq = fi & 3;
        const int q4 = lane & 3, p16 = lane >> 2;
        const int col = bb * DM + ub * 64 + g * 16 + cq * 4 + q4;
        const u32 xv = *(const GAS u16*)(a.x + col);
        GAS u16* po = (GAS u16*)(a.out + col);
        float xf_ = bflo(xv);
        PIN(po); PIN(xf_);
        lds_wait_ge(SY + C_PUB2, 2);
        __builtin_amdgcn_s_sleep(RDELAY);
        const int o2 = WS_MB2 + ((bb * 16 + p16) * 8 + ub) * 256 + (cq * 4 + q4) * 16;
        const int o3 = WS_MB3 + (((bb * 8 + ub) * 2 + (cq >> 1)) * 56 + p16) * 128 + (cq & 1) * 64 + q4 * 16;
        v4u at, a0, a1, a2, a3 = v4u{tg, tg, tg, tg};
        watch3(a.ws + WS_M3 + ((fi >> M3S) * 4 + q4) * 128, SY[W_B + 1 + q4], rs, o2, o3, at, a0, a1, a2, a3);
        STAMP(17);
        at ^= tg;
        if (__builtin_expect(!__all(ok4(at)), 0)) at = poll1(rs, tg, o2);
        settle<2048, 28>(L, lane, rs, o3, tg, a0, a1, a2, a3);
        float t = rscatter(F4(at));
        t += dpp<ROR4>(t);
        t += dpp<XOR8>(t);
        const float hv = rbf(xf_ + rbf(t));
        const f4 ds = (F4(a0) + F4(a1)) + (F4(a2) + F4(a3));
        t = rscatter(ds);
        t += dpp<ROR4>(t);
        t += dpp<XOR8>(t);
        const float ov = hv + rbf(t);
        if (c < 4) *po = (u16)cvtpk(ov, ov);
        if (w == 0 && lane == 0) __hip_atomic_store((GAS u64*)(a.ws + WS_EPOCH), *(LAS u64*)(SY + W_E), __ATOMIC_RELAXED, __HIP_MEMORY_SCOPE_AGENT);
        STAMP(9);
      }
      if (u >= 4) {
        const int lam = u - 4;
        lds_wait_ge(SY + C_VISS, 8);
        __builtin_amdgcn_s_setprio(LPRIO);
        const __amdgpu_buffer_rsrc_t rw = mkrsrc(a.wo, 4096 * DM * 2);
        const u32 vo = (u32)(((r8 & 3) * 128 + 8 * (r8 >> 2)) * 1024 + wjj * 128);
        dma_run2<1>(rw, vo + swA, vo + swB, (u32)((x8 * 512 + 2 * lam + hw) * 1024), 16 * 1024, (u32)(size_t)(L + L_WO) + lam * 8192, 1024, 4);
        VM0();
        lds_add(SY + C_W, lane);
        if (PFM) { lds_wait_ge(SY + C_PUB, 1); if (__builtin_popcount(__builtin_amdgcn_readfirstlane(SY[F_GO])) < TS) PF2(); }
      }
    } else if (isFFN) {
      if (u >= 4) {
        lds_wait_ge(SY + C_VISS, 8);
        __builtin_amdgcn_s_setprio(LPRIO);
        if (FGATE == 1) lds_wait_ge(SY + C_O, 7);
        if (FGATE == 2) lds_wait_ge(SY + C_PUB, 1);
        if (PFM & 4) { const int lam = u - 4; PF2(); }
        const int lam = u - 4;
        const __amdgpu_buffer_rsrc_t rg = mkrsrc(a.wg, DM * FF * 2), ru = mkrsrc(a.wu, DM * FF * 2), rd = mkrsrc(a.wd, FF * DM * 2);
        const int r16 = lane >> 2, q4 = lane & 3;
        const u32 vg = (u32)(r16 * FF * 2 + ff * 64 + ((q4 ^ ((r16 >> 3) << 1)) * 16));
        u32 mode = 0;
#define PACE() do { if (mode == 0) { asm volatile("s_waitcnt vmcnt(%0)" :: "n"(WD1 - 1) : "memory"); \
                      mode = __builtin_amdgcn_readfirstlane(__hip_atomic_load(SY + C_PUB, __ATOMIC_RELAXED, __HIP_MEMORY_SCOPE_WORKGROUP)) >= 1; } } while (0)
#define DNLOAD(t) do { const u32 k = lam * 8 + (((t) + 4 * fb) & 7); const u32 tau = (k & 3) | (((k >> 3) & 1) << 2); PACE(); \
          bl_lds(rd, (u32)((((lane >> 1) ^ tau) * 2 + (lane & 1)) * 16), (ff * 32 + k) * 1024, (u32)(size_t)(L + L_DN) + k * 1024); } while (0)
        _Pragma("clang loop unroll(disable)") for (int t = 0; t < 8; t++) {
          const u32 P = lam * 8 + ((t + 2 * (fidx & 3)) & 7);
          PACE();
          bl_lds(rg, vg, P * (16 * FF * 2), (u32)(size_t)(L + L_GU) + P * 1024);
          PACE();
          bl_lds(ru, vg, P * (16 * FF * 2), (u32)(size_t)(L + L_GU) + 32768 + P * 1024);
        }
        _Pragma("clang loop unroll(disable)") for (int t = 0; t < 8; t++) DNLOAD(t);
        asm volatile("s_waitcnt vmcnt(8)" ::: "memory");
        lds_set(SY + F_K + lam, lane, 5);
        STAMP(21);
        if (LBAR) wg_barrier();
        VM0();
        lds_add(SY + C_W, lane);
        STAMP(15);
      } else {
        if (u == 3) {
          lds_wait_ge(SY + C_PUB, 1);
          __builtin_amdgcn_s_sleep(PPDELAY);
          watch<EX2 ? 0 : 1, EX2 ? 0xFFFF : -1>(a.ws + mw, SY[W_B], (u32)(size_t)(SY + F_GO));
        }
      }
    } else if (PFM && u >= 4) {
      const int lam = u - 4;
      lds_wait_ge(SY + C_PUB, 1);
      PF2();
    }
    VM0();
  }
  FLUSH_TS();
}

extern "C" void kernel_launch(void* const* d_in, const int* in_sizes, int n_in, void* d_out, int out_size,
                              void* d_ws, size_t ws_size, hipStream_t stream) {
  Args a;
  a.ws = (char*)d_ws;
  a.x = (const u16*)d_in[0];
  a.k = (const u16*)d_in[1];
  a.v = (const u16*)d_in[2];
  a.wq = (const u16*)d_in[3];
  a.wo = (const u16*)d_in[4];
  a.wg = (const u16*)d_in[6];
  a.wu = (const u16*)d_in[7];
  a.wd = (const u16*)d_in[8];
  a.out = (u16*)d_out;
  hipLaunchKernelGGL(fused_block, dim3(NWG), dim3(NT), 0, stream, a);
}
```
